```python
import torch

def _softmax(x, axis=-1):
    return torch.softmax(x, dim=axis)
def _gelu(x):
    return torch.nn.functional.gelu(x, approximate="tanh")
def _silu(x):
    return torch.nn.functional.silu(x)
def _sigmoid(x):
    return torch.sigmoid(x)
def _layernorm(x, axis=-1, eps=1e-5):
    m = x.mean(dim=axis, keepdim=True)
    v = x.var(dim=axis, keepdim=True, unbiased=False)
    return (x - m) / torch.sqrt(v + eps)
def _scatter_add(tgt, idx, src, axis=0):
    return tgt.clone().index_add_(axis, idx, src)
def _segment_sum(src, seg, n):
    out = torch.zeros((n,) + src.shape[1:], dtype=src.dtype)
    return out.index_add_(0, seg, src)
def _pad(x, pw):
    flat = tuple(v for pair in reversed(pw) for v in pair)
    return torch.nn.functional.pad(x, flat)

kernel_name = 'parallel_sum__sliding_block_cached__glu_gated_glu__decode'

def setup_inputs():

    _w = (lambda a: (lambda t: t.to(torch.bfloat16) if t.is_floating_point() else t)(torch.as_tensor(a)))
    def _A_setup():
        B, Sq, D = 2, 1, 512
        Hq, Hkv, Sk, Dh = 32, 32, 4096, 128
        assert Hq % Hkv == 0, (Hq, Hkv)
        return {
            "x": _w((lambda *s: torch.randn(*s, dtype=torch.bfloat16) * 0.05)(B, Sq, D)),
            "k_cache": _w((lambda *s: torch.randn(*s, dtype=torch.bfloat16) * 3.2)(B, Hkv, Sk, Dh)),
            "v_cache": _w((lambda *s: torch.randn(*s, dtype=torch.bfloat16) * 1.6)(B, Hkv, Sk, Dh)),
            "wq": _w((lambda *s: torch.randn(*s, dtype=torch.bfloat16) * 3.2)(D, Hq * Dh) * (D ** -0.5)),
            "wo": _w((lambda *s: torch.randn(*s, dtype=torch.bfloat16) * 1.6)(Hq * Dh, D) * ((Hq * Dh) ** -0.5)),
        }
    _A_in = _A_setup()

    def _B_setup():
        B, S, D, F = 2, 1, 512, 1376
        return {
            "x": _w((lambda *s: torch.randn(*s, dtype=torch.bfloat16) * 0.05)(B, S, D)),
            "w_gate": _w((lambda *s: torch.randn(*s, dtype=torch.bfloat16) * 0.2)(D, F)),
            "w_up": _w((lambda *s: torch.randn(*s, dtype=torch.bfloat16) * 0.05)(D, F)),
            "w_down": _w((lambda *s: torch.randn(*s, dtype=torch.bfloat16) * 0.05)(F, D)),
        }
    _B_in = _B_setup()

    return {
        "A_x": _A_in["x"],
        "A_k_cache": _A_in["k_cache"],
        "A_v_cache": _A_in["v_cache"],
        "A_wq": _A_in["wq"],
        "A_wo": _A_in["wo"],
        "B_x": _B_in["x"],
        "B_w_gate": _B_in["w_gate"],
        "B_w_up": _B_in["w_up"],
        "B_w_down": _B_in["w_down"],
    }

def reference(A_x, A_k_cache, A_v_cache, A_wq, A_wo, B_x, B_w_gate, B_w_up, B_w_down):
    def _A_ref(x, k_cache, v_cache, wq, wo):
        B, Sq, D = x.shape
        Hq, Dh = 32, 128
        Hkv, Sk = k_cache.shape[1], k_cache.shape[2]
        W = min(4096, max(256, Sk // 4))
        rep = Hq // Hkv
        q = (lambda x, *d: x.permute(*d))((x @ wq).reshape(B, Sq, Hq, Dh), 0, 2, 1, 3)
        k = (lambda x, n, axis: torch.repeat_interleave(x, n, dim=axis))(k_cache[:, :, Sk - W:, :], rep, axis=1)
        v = (lambda x, n, axis: torch.repeat_interleave(x, n, dim=axis))(v_cache[:, :, Sk - W:, :], rep, axis=1)
        scores = torch.einsum("bhqd,bhkd->bhqk", q, k) * (Dh ** -0.5)
        p = _softmax(scores, axis=-1)
        o = torch.einsum("bhqk,bhkd->bhqd", p, v)
        return (lambda x, *d: x.permute(*d))(o, 0, 2, 1, 3).reshape(B, Sq, Hq * Dh) @ wo

    def _B_ref(x, w_gate, w_up, w_down):
        g = torch.einsum("bsd,df->bsf", x, w_gate)
        u = torch.einsum("bsd,df->bsf", x, w_up)
        h = _sigmoid(g) * u
        return torch.einsum("bsf,fd->bsd", h, w_down)

    x = A_x
    xn = _layernorm(x, axis=-1)
    return x + _A_ref(xn, A_k_cache, A_v_cache, A_wq, A_wo) + _B_ref(xn, B_w_gate, B_w_up, B_w_down)
```

```cpp
#include <hip/hip_runtime.h>
#include <hip/hip_bf16.h>
#include <stdint.h>

typedef unsigned short u16;
__device__ __forceinline__ float bf2f(u16 v) { return __uint_as_float(((unsigned)v) << 16); }
__device__ __forceinline__ u16 f2bf(float f) { __bf16 b = (__bf16)f; return __builtin_bit_cast(u16, b); }
__device__ __forceinline__ float rbf(float f) { return bf2f(f2bf(f)); }

constexpr int D = 512, H = 32, DH = 128, SK = 4096, W = 1024, F = 1376;

__device__ void layernorm(const u16* x, float* xn, float* red) {
  int t = threadIdx.x;
  for (int b = 0; b < 2; ++b) {
    float a0 = bf2f(x[b * D + t]), a1 = bf2f(x[b * D + t + 256]);
    red[t] = a0 + a1; __syncthreads();
    for (int s = 128; s > 0; s >>= 1) { if (t < s) red[t] += red[t + s]; __syncthreads(); }
    float mean = red[0] / 512.f; __syncthreads();
    float d0 = a0 - mean, d1 = a1 - mean;
    red[t] = d0 * d0 + d1 * d1; __syncthreads();
    for (int s = 128; s > 0; s >>= 1) { if (t < s) red[t] += red[t + s]; __syncthreads(); }
    float var = red[0] / 512.f; __syncthreads();
    float m = rbf(mean), v = rbf(var);
    float den = rbf(sqrtf(rbf(v + 1e-5f)));
    xn[b * D + t] = rbf(rbf(a0 - m) / den);
    xn[b * D + t + 256] = rbf(rbf(a1 - m) / den);
  }
  __syncthreads();
}

__global__ void k1(const u16* x, const u16* wq, const u16* wg, const u16* wu, float* q, float* h) {
  __shared__ float xn[2 * D]; __shared__ float red[256];
  layernorm(x, xn, red);
  int c = blockIdx.x * 256 + threadIdx.x;
  if (c < H * DH) {
    float a0 = 0, a1 = 0;
    for (int k = 0; k < D; ++k) { float w = bf2f(wq[(size_t)k * (H * DH) + c]); a0 += xn[k] * w; a1 += xn[D + k] * w; }
    q[c] = rbf(a0); q[H * DH + c] = rbf(a1);
  } else if (c < H * DH + F) {
    int f = c - H * DH;
    float g0 = 0, g1 = 0, u0 = 0, u1 = 0;
    for (int k = 0; k < D; ++k) { float w = bf2f(wg[(size_t)k * F + f]); float v = bf2f(wu[(size_t)k * F + f]);
      g0 += xn[k] * w; g1 += xn[D + k] * w; u0 += xn[k] * v; u1 += xn[D + k] * v; }
    g0 = rbf(g0); g1 = rbf(g1); u0 = rbf(u0); u1 = rbf(u1);
    float s0 = rbf(1.f / (1.f + __expf(-g0))), s1 = rbf(1.f / (1.f + __expf(-g1)));
    h[f] = rbf(s0 * u0); h[F + f] = rbf(s1 * u1);
  }
}

__global__ void k2(const float* q, const u16* kc, const u16* vc, float* o) {
  __shared__ float s[W]; __shared__ float red[256]; __shared__ float qs[DH];
  int bh = blockIdx.x; int b = bh / H, hh = bh % H; int t = threadIdx.x;
  if (t < DH) qs[t] = q[b * H * DH + hh * DH + t];
  __syncthreads();
  const u16* K = kc + ((size_t)bh * SK + (SK - W)) * DH;
  const u16* V = vc + ((size_t)bh * SK + (SK - W)) * DH;
  float mx = -INFINITY;
  for (int j = t; j < W; j += 256) {
    float a = 0; for (int d = 0; d < DH; ++d) a += qs[d] * bf2f(K[(size_t)j * DH + d]);
    a = rbf(rbf(a) * 0.08838834764831845f);
    s[j] = a; mx = fmaxf(mx, a);
  }
  red[t] = mx; __syncthreads();
  for (int st = 128; st > 0; st >>= 1) { if (t < st) red[t] = fmaxf(red[t], red[t + st]); __syncthreads(); }
  mx = red[0]; __syncthreads();
  float sum = 0;
  for (int j = t; j < W; j += 256) { float e = __expf(s[j] - mx); s[j] = e; sum += e; }
  red[t] = sum; __syncthreads();
  for (int st = 128; st > 0; st >>= 1) { if (t < st) red[t] += red[t + st]; __syncthreads(); }
  sum = red[0]; __syncthreads();
  for (int j = t; j < W; j += 256) s[j] = rbf(s[j] / sum);
  __syncthreads();
  if (t < DH) { float a = 0; for (int j = 0; j < W; ++j) a += s[j] * bf2f(V[(size_t)j * DH + t]); o[b * H * DH + hh * DH + t] = rbf(a); }
}

__global__ void k3(const u16* x, const float* o, const float* h, const u16* wo, const u16* wd, u16* out) {
  __shared__ float red[4][256];
  int t = threadIdx.x; int c = blockIdx.x * 16 + (t & 15); int r = t >> 4;
  float a0 = 0, a1 = 0, b0 = 0, b1 = 0;
  for (int k = r; k < H * DH; k += 16) { float w = bf2f(wo[(size_t)k * D + c]); a0 += o[k] * w; a1 += o[H * DH + k] * w; }
  for (int k = r; k < F; k += 16) { float w = bf2f(wd[(size_t)k * D + c]); b0 += h[k] * w; b1 += h[F + k] * w; }
  red[0][t] = a0; red[1][t] = a1; red[2][t] = b0; red[3][t] = b1; __syncthreads();
  if (t < 32) { int bb = t >> 4, cc = t & 15; float A = 0, B = 0;
    for (int i = 0; i < 16; ++i) { A += red[bb][i * 16 + cc]; B += red[2 + bb][i * 16 + cc]; }
    int col = blockIdx.x * 16 + cc;
    float r1 = rbf(bf2f(x[bb * D + col]) + rbf(A));
    out[bb * D + col] = f2bf(r1 + rbf(B)); }
}

extern "C" void kernel_launch(void* const* d_in, const int* in_sizes, int n_in,
                              void* d_out, int out_size, void* d_ws, size_t ws_size,
                              hipStream_t stream) {
  const u16* x = (const u16*)d_in[0]; const u16* kc = (const u16*)d_in[1]; const u16* vc = (const u16*)d_in[2];
  const u16* wq = (const u16*)d_in[3]; const u16* wo = (const u16*)d_in[4];
  const u16* wg = (const u16*)d_in[6]; const u16* wu = (const u16*)d_in[7]; const u16* wd = (const u16*)d_in[8];
  float* q = (float*)d_ws; float* h = q + 2 * H * DH; float* o = h + 4096;
  k1<<<(H * DH + F + 255) / 256, 256, 0, stream>>>(x, wq, wg, wu, q, h);
  k2<<<64, 256, 0, stream>>>(q, kc, vc, o);
  k3<<<D / 16, 256, 0, stream>>>(x, o, h, wo, wd, (u16*)d_out);
}
```

```cpp
#include <hip/hip_runtime.h>
#include <hip/hip_bf16.h>
#include <stdint.h>

typedef unsigned short u16;
typedef unsigned u32;
typedef u32 u32x4 __attribute__((ext_vector_type(4)));
typedef u32 u32x2 __attribute__((ext_vector_type(2)));
typedef float f32x4 __attribute__((ext_vector_type(4)));
typedef float f32x2 __attribute__((ext_vector_type(2)));
typedef __bf16 bf16x2 __attribute__((ext_vector_type(2)));
typedef __bf16 bf16x8 __attribute__((ext_vector_type(8)));
#define GAS __attribute__((address_space(1)))
#define LAS __attribute__((address_space(3)))

constexpr int D = 512, NH = 32, DH = 128, SK = 4096, WIN = 1024, FF = 1376;
constexpr int NTHR = 640;
#define KWIN 6
#define VWIN 6
#define KSIG 3
#define KQT 15
#define FWIN 4
constexpr int AUX_NT = 0, AUX_SC1 = 16;
constexpr u32 SPIN_CAP = 1u << 13;
constexpr u32 LSPIN_CAP = 1u << 17;

constexpr u32 WS_E = 0;
constexpr u32 WS_X1 = 4096;
constexpr u32 WS_X2 = WS_X1 + 32 * 8 * 2 * 1024;
constexpr u32 WS_X3 = WS_X2 + 32 * 8 * 2048;
constexpr u32 WS_END = WS_X3 + 8 * 32 * 1024;

constexpr int L_QF = 0;
constexpr int L_FF = 1;
constexpr int L_KQ = 2;
constexpr int L_PF = 4;
constexpr int L_MX = 8;
constexpr int L_KI = 16;
constexpr int L_PR = 20;
constexpr int L_VI = 24;
constexpr int L_QPK = 32;
constexpr int L_HPK = 96;
constexpr int L_OPK = 160;
constexpr int L_YF = 288;
constexpr int L_PL = 416;
constexpr int L_P = 432;
constexpr int L_PART = 688;
constexpr int L_DW = L_PART + 512;

__device__ __forceinline__ float bflo(u32 v) { return __uint_as_float(v << 16); }
__device__ __forceinline__ float bfhi(u32 v) { return __uint_as_float(v & 0xffff0000u); }
__device__ __forceinline__ u32 pk2(float lo, float hi) {
  bf16x2 r = {(__bf16)lo, (__bf16)hi};
  return __builtin_bit_cast(u32, r);
}
__device__ __forceinline__ float rbf(float f) { return bflo(pk2(f, f)); }
template <int CTRL> __device__ __forceinline__ float dpp(float x) {
  return __builtin_bit_cast(float, __builtin_amdgcn_mov_dpp(__builtin_bit_cast(int, x), CTRL, 0xf, 0xf, true));
}
constexpr int XOR1 = 0xB1, XOR2 = 0x4E, XOR7 = 0x141, ROR8 = 0x128, ROR4 = 0x124;
__device__ __forceinline__ float xrow_sum(float x) {
  auto s = __builtin_amdgcn_permlane16_swap(__float_as_uint(x), __float_as_uint(x), false, false);
  x = __uint_as_float(s[0]) + __uint_as_float(s[1]);
  auto t = __builtin_amdgcn_permlane32_swap(__float_as_uint(x), __float_as_uint(x), false, false);
  return __uint_as_float(t[0]) + __uint_as_float(t[1]);
}
__device__ __forceinline__ float xrow_max(float x) {
  auto s = __builtin_amdgcn_permlane16_swap(__float_as_uint(x), __float_as_uint(x), false, false);
  x = fmaxf(__uint_as_float(s[0]), __uint_as_float(s[1]));
  auto t = __builtin_amdgcn_permlane32_swap(__float_as_uint(x), __float_as_uint(x), false, false);
  return fmaxf(__uint_as_float(t[0]), __uint_as_float(t[1]));
}
__device__ __forceinline__ float wave_sum(float x) {
  x += dpp<XOR1>(x); x += dpp<XOR2>(x); x += dpp<XOR7>(x); x += dpp<ROR8>(x);
  return xrow_sum(x);
}
__device__ __forceinline__ float wave_max(float x) {
  x = fmaxf(x, dpp<XOR1>(x)); x = fmaxf(x, dpp<XOR2>(x)); x = fmaxf(x, dpp<XOR7>(x)); x = fmaxf(x, dpp<ROR8>(x));
  return xrow_max(x);
}
__device__ __forceinline__ float dot2(u32 a, u32 b, float c) {
  return __builtin_amdgcn_fdot2_f32_bf16(__builtin_bit_cast(bf16x2, a), __builtin_bit_cast(bf16x2, b), c, false);
}
typedef __amdgpu_buffer_rsrc_t rsrc_t;
__device__ __forceinline__ rsrc_t mk_rsrc(const void* p, u32 bytes) {
  return __builtin_amdgcn_make_buffer_rsrc((void*)p, 0, bytes, 0x00020000);
}
__device__ __forceinline__ u32x4 ld_nt(rsrc_t r, u32 voff, u32 soff) { return __builtin_amdgcn_raw_buffer_load_b128(r, voff, soff, AUX_NT); }
__device__ __forceinline__ u32x4 ld_pl(rsrc_t r, u32 voff, u32 soff) { return __builtin_amdgcn_raw_buffer_load_b128(r, voff, soff, 0); }
__device__ __forceinline__ u32x4 ld_sc(rsrc_t r, u32 voff, u32 soff) { return __builtin_amdgcn_raw_buffer_load_b128(r, voff, soff, AUX_SC1); }
__device__ __forceinline__ void st_unit(rsrc_t r, u32 voff, float a, float b, u32 tag) {
  u32x4 v = {__float_as_uint(a), tag, __float_as_uint(b), tag};
  __builtin_amdgcn_raw_buffer_store_b128(v, r, voff, 0, AUX_SC1);
}
__device__ __forceinline__ f32x4 mfma(u32x4 a, u32x4 b, f32x4 c) {
  return __builtin_amdgcn_mfma_f32_16x16x32_bf16(__builtin_bit_cast(bf16x8, a), __builtin_bit_cast(bf16x8, b), c, 0, 0, 0);
}
template <int N> __device__ __forceinline__ void waitv() { asm volatile("s_waitcnt vmcnt(%0)" :: "i"(N) : "memory"); }
#define CBAR() asm volatile("" ::: "memory")
#define SCHEDB() __builtin_amdgcn_sched_barrier(0)
__device__ __forceinline__ u32 lds_ld(volatile LAS u32* p) { return *p; }

#define STAMP(i) do {} while (0)
#define STFLUSH() do {} while (0)
#define STDECL() do {} while (0)

__device__ __forceinline__ void tr8(const u32x4* W, u32x4* Bf) {
#pragma unroll
  for (int q = 0; q < 4; ++q) {
#pragma unroll
    for (int p = 0; p < 4; ++p) {
      Bf[2 * q][p] = __builtin_amdgcn_perm(W[2 * p + 1][q], W[2 * p][q], 0x05040100u);
      Bf[2 * q + 1][p] = __builtin_amdgcn_perm(W[2 * p + 1][q], W[2 * p][q], 0x07060302u);
    }
  }
}
__device__ __forceinline__ void epi(const f32x4* acc, int lane, float& e0, float& e1) {
  const bool lo = (lane & 8) == 0;
  float v[8];
#pragma unroll
  for (int j = 0; j < 8; ++j) {
    const float send = lo ? acc[j][2] : acc[j][1];
    const float keep = lo ? acc[j][0] : acc[j][3];
    v[j] = keep + dpp<ROR8>(send);
  }
  const int grp = lane >> 4;
  e0 = grp == 0 ? v[0] : grp == 1 ? v[2] : grp == 2 ? v[4] : v[6];
  e1 = grp == 0 ? v[1] : grp == 1 ? v[3] : grp == 2 ? v[5] : v[7];
}

__global__ void __launch_bounds__(NTHR, 3)
fused_decode(const u16* __restrict__ x, const u16* __restrict__ kc, const u16* __restrict__ vc,
             const u16* __restrict__ wq, const u16* __restrict__ wo, const u16* __restrict__ wg,
             const u16* __restrict__ wu, const u16* __restrict__ wd, u16* __restrict__ out, unsigned char* __restrict__ ws) {
  extern __shared__ __attribute__((aligned(16))) u32 lds_raw[];
  LAS u32* lds = (LAS u32*)lds_raw;
  const int tid = threadIdx.x, lane = tid & 63;
  const int wave = __builtin_amdgcn_readfirstlane(tid >> 6);
  STDECL();
  lds[tid & 31] = 0u;
  asm volatile("s_waitcnt lgkmcnt(0)" :: "s"(x), "s"(kc), "s"(vc), "s"(wq), "s"(wo), "s"(wg), "s"(wu), "s"(wd), "s"(out), "s"(ws) : "memory");
  STAMP(0);
  const int g = blockIdx.x;
  const int h = (g & 7) * 4 + (g >> 6), j = (g >> 3) & 7;

  if (wave >= 2) {
    const int sw = (wave - 2) & 3;
    const int b = j >> 2, s = j & 3;
    const size_t kvoff = ((size_t)(b * NH + h) * SK + (SK - WIN) + 256 * s + 64 * sw) * DH;
    const u32 vo = lane * 16;
    const int c = lane & 15;
    if (wave < 6) {
      const rsrc_t rk = mk_rsrc(kc + kvoff, 16384);
      u32x4 K[16];
      asm volatile("s_barrier" ::: "memory");
      SCHEDB();
#pragma unroll
      for (int t = 0; t < 16; ++t) {
        if (t >= KWIN) waitv<KWIN - 1>();
        SCHEDB();
        K[t] = ld_nt(rk, vo, t * 1024);
        SCHEDB();
        if (t == KQT) { if (sw == 0 && lane == 0) *(volatile LAS u32*)(lds + L_KQ) = 1u; SCHEDB(); }
      }
      waitv<KSIG>();
      if (lane == 0) *(volatile LAS u32*)(lds + L_KI + sw) = 1u;
      STAMP(1);
      for (u32 sp = 0; lds_ld(lds + L_QF) == 0u && sp < LSPIN_CAP; ++sp) __builtin_amdgcn_s_sleep(1);
      CBAR();
      STAMP(2);
      const u32x4 q = *(LAS u32x4*)(lds + L_QPK + 4 * c);
      float xs[16];
#pragma unroll
      for (int t = 0; t < 16; ++t) {
        float d = dot2(K[t][0], q[0], 0.f);
        d = dot2(K[t][1], q[1], d); d = dot2(K[t][2], q[2], d); xs[t] = dot2(K[t][3], q[3], d);
      }
#define TR_STEP(HALF, CTRL, BIT) _Pragma("unroll") for (int u = 0; u < (HALF); ++u) { \
        const bool up_ = (c & (BIT)) != 0; const float keep_ = up_ ? xs[u + (HALF)] : xs[u], send_ = up_ ? xs[u] : xs[u + (HALF)]; \
        xs[u] = keep_ + dpp<CTRL>(send_); }
      TR_STEP(8, ROR8, 8) TR_STEP(4, XOR7, 4) TR_STEP(2, XOR2, 2) TR_STEP(1, XOR1, 1)
#undef TR_STEP
      const float sc = rbf(rbf(xs[0]) * 0.08838834764831845f);
      const float mw = wave_max(sc);
      if (lane == 0) { *(LAS u32x2*)(lds + L_MX + 2 * sw) = u32x2{__float_as_uint(mw), 1u}; }
      STAMP(3);
      float M;
      for (u32 sp = 0;; ++sp) {
        CBAR();
        const u32x4 a = *(volatile LAS u32x4*)(lds + L_MX), bq = *(volatile LAS u32x4*)(lds + L_MX + 4);
        M = fmaxf(fmaxf(__uint_as_float(a[0]), __uint_as_float(a[2])), fmaxf(__uint_as_float(bq[0]), __uint_as_float(bq[2])));
        if ((a[1] & a[3] & bq[1] & bq[3]) != 0u || sp >= LSPIN_CAP) break;
      }
      const float p = __builtin_amdgcn_exp2f((sc - M) * 1.4426950408889634f);
      lds[L_P + 64 * sw + lane] = __float_as_uint(p);
      CBAR();
      if (lane == 0) *(volatile LAS u32*)(lds + L_PR + sw) = 1u;
      const float lw = wave_sum(p);
      if (lane == 0) lds[L_PL + sw] = __float_as_uint(lw);
      STAMP(4);
      STFLUSH();
      return;
    }
    const rsrc_t rv = mk_rsrc(vc + kvoff, 16384);
    u32x4 V[16];
    asm volatile("s_barrier" ::: "memory");
    for (u32 sp = 0; lds_ld(lds + L_KI + sw) == 0u && sp < LSPIN_CAP; ++sp) __builtin_amdgcn_s_sleep(2);
    SCHEDB();
    STAMP(1);
#pragma unroll
    for (int t = 0; t < 16; ++t) {
      if (t >= VWIN) waitv<VWIN - 1>();
      SCHEDB();
      V[t] = ld_nt(rv, vo, t * 1024);
      SCHEDB();
    }
    if (lane == 0) *(volatile LAS u32*)(lds + L_VI + sw) = 1u;
    STAMP(2);
    for (u32 sp = 0; lds_ld(lds + L_PR + sw) == 0u && sp < LSPIN_CAP; ++sp) __builtin_amdgcn_s_sleep(1);
    CBAR();
    const float p = __uint_as_float(lds[L_P + 64 * sw + lane]);
    STAMP(3);
    float acc[8];
#pragma unroll
    for (int k = 0; k < 8; ++k) acc[k] = 0.f;
#define PV_STEP(T) { const float pb = dpp<0x150 + (T)>(p); \
      _Pragma("unroll") for (int k = 0; k < 4; ++k) { acc[2 * k] = fmaf(pb, bflo(V[T][k]), acc[2 * k]); acc[2 * k + 1] = fmaf(pb, bfhi(V[T][k]), acc[2 * k + 1]); } }
    PV_STEP(0) PV_STEP(1) PV_STEP(2) PV_STEP(3) PV_STEP(4) PV_STEP(5) PV_STEP(6) PV_STEP(7)
    PV_STEP(8) PV_STEP(9) PV_STEP(10) PV_STEP(11) PV_STEP(12) PV_STEP(13) PV_STEP(14) PV_STEP(15)
#undef PV_STEP
    float v1[4];
#pragma unroll
    for (int k = 0; k < 4; ++k) {
      auto sw2 = __builtin_amdgcn_permlane32_swap(__float_as_uint(acc[k]), __float_as_uint(acc[k + 4]), false, false);
      v1[k] = __uint_as_float(sw2[0]) + __uint_as_float(sw2[1]);
    }
    auto s0 = __builtin_amdgcn_permlane16_swap(__float_as_uint(v1[0]), __float_as_uint(v1[2]), false, false);
    auto s1 = __builtin_amdgcn_permlane16_swap(__float_as_uint(v1[1]), __float_as_uint(v1[3]), false, false);
    const float o0 = __uint_as_float(s0[0]) + __uint_as_float(s0[1]), o1 = __uint_as_float(s1[0]) + __uint_as_float(s1[1]);
    *(LAS f32x2*)(lds + L_PART + 128 * sw + 8 * c + 2 * (lane >> 4)) = f32x2{o0, o1};
    CBAR();
    if (lane == 0) *(volatile LAS u32*)(lds + L_PF + sw) = 1u;
    STAMP(4);
    STFLUSH();
    return;
  }

  __builtin_amdgcn_s_setprio(3);
  const int f = wave;
  const rsrc_t rws = mk_rsrc(ws, WS_END);
  const int m4 = lane & 3, gq = lane >> 4, rho = lane >> 3, chunk = lane & 7;
  const int ab = (m4 >> 1), ahi = m4 & 1;
  const int kq = j >> 1;
  const int nch = h < 12 ? 6 : 5, c0 = h < 12 ? 6 * h : 72 + 5 * (h - 12);
  const rsrc_t rx = mk_rsrc(x, 2048);
  const u32x4 xs0 = ld_pl(rx, lane * 16, 0), xs1 = ld_pl(rx, lane * 16, 1024);
  const u32 xaoff = ab * 1024 + (128 * kq + 16 * gq + 8 * ahi) * 2;
  const u32x4 xa0 = ld_pl(rx, xaoff, 0), xa1 = ld_pl(rx, xaoff, 128);
  const u32 E = __builtin_amdgcn_raw_buffer_load_b32(rws, WS_E, 0, AUX_SC1);
  const u32 xfin = __builtin_amdgcn_raw_buffer_load_b32(rx, (lane & 1) * 1024 + (64 * j + 2 * h) * 2, 0, 0);
  u32x4 W[16];
  {
    const u16* pa = f == 0 ? wq + (size_t)(128 * kq) * 4096 + h * 128 + 64 * (j & 1)
                           : ((j & 1) ? wu : wg) + (size_t)(128 * kq) * FF + 8 * c0;
    const u32 strb = f == 0 ? 8192u : 2752u;
    const int ch = f == 0 ? chunk : (chunk < nch ? chunk : nch - 1);
    const rsrc_t ra = mk_rsrc(pa, 128u * strb);
    const u32 vo = rho * 8 * strb + ch * 16;
#pragma unroll
    for (int t = 0; t < 16; ++t) {
      W[t] = ld_nt(ra, vo, ((t >> 3) * 64 + (t & 7)) * strb);
      SCHEDB(); }
    asm volatile("s_barrier" ::: "memory");
  }
  SCHEDB();
  STAMP(1);
  float mb, rd;
  {
    float s0 = 0.f, s1 = 0.f;
#pragma unroll
    for (int k = 0; k < 4; ++k) { s0 += bflo(xs0[k]) + bfhi(xs0[k]); s1 += bflo(xs1[k]) + bfhi(xs1[k]); }
    const float mean0 = wave_sum(s0) * (1.f / 512.f), mean1 = wave_sum(s1) * (1.f / 512.f);
    float q0 = 0.f, q1 = 0.f;
#pragma unroll
    for (int k = 0; k < 4; ++k) {
      float a = bflo(xs0[k]) - mean0, bb = bfhi(xs0[k]) - mean0; q0 = fmaf(a, a, q0); q0 = fmaf(bb, bb, q0);
      a = bflo(xs1[k]) - mean1; bb = bfhi(xs1[k]) - mean1; q1 = fmaf(a, a, q1); q1 = fmaf(bb, bb, q1);
    }
    const float var0 = wave_sum(q0) * (1.f / 512.f), var1 = wave_sum(q1) * (1.f / 512.f);
    const float mean = ab ? mean1 : mean0, var = ab ? var1 : var0;
    mb = rbf(mean);
    rd = rbf(__builtin_amdgcn_sqrtf(rbf(rbf(var) + 1e-5f)));
  }
  const float rinv = __builtin_amdgcn_rcpf(rd);
  u32x4 A0, A1;
#pragma unroll
  for (int k = 0; k < 4; ++k) {
    float v[4] = {rbf(bflo(xa0[k]) - mb), rbf(bfhi(xa0[k]) - mb), rbf(bflo(xa1[k]) - mb), rbf(bfhi(xa1[k]) - mb)};
#pragma unroll
    for (int e = 0; e < 4; ++e) { const float q0 = v[e] * rinv; const float r = fmaf(-q0, rd, v[e]); v[e] = fmaf(r, rinv, q0); }
    A0[k] = pk2(v[0], v[1]); A1[k] = pk2(v[2], v[3]);
  }
  STAMP(2);
  const u32 tag = E + 1u;
  float e0, e1;
  {
    f32x4 acc[8];
    u32x4 Bf[8];
    tr8(W, Bf);
#pragma unroll
    for (int t = 0; t < 8; ++t) acc[t] = mfma(A0, Bf[t], f32x4{0.f, 0.f, 0.f, 0.f});
    tr8(W + 8, Bf);
#pragma unroll
    for (int t = 0; t < 8; ++t) acc[t] = mfma(A1, Bf[t], acc[t]);
    epi(acc, lane, e0, e1);
  }
  STAMP(13);
  const u32 uidx = ((lane >> 3) & 1) * 32 + 4 * (lane & 7) + (lane >> 4);
  st_unit(rws, WS_X1 + ((h * 8 + j) * 2 + f) * 1024 + uidx * 16, e0, e1, tag);
  STAMP(3);
  if (f == 1) {
    {
      const u16* pb = wd + (size_t)(8 * c0) * D + 64 * j;
      const int rr = rho < nch ? rho : nch - 1;
      const rsrc_t rb = mk_rsrc(pb, 64u * 1024u);
      const u32 vo = rr * 8192 + chunk * 16;
#pragma unroll
      for (int t = 0; t < 8; ++t) { W[t] = ld_nt(rb, vo, t * 1024); SCHEDB(); }
    }
    float a0x, a0y, a1x, a1y;
    for (u32 sp = 0; lds_ld(lds + L_KI) == 0u && sp < LSPIN_CAP; ++sp) __builtin_amdgcn_s_sleep(2);
    for (u32 sp = 0;; ++sp) {
      CBAR();
      u32x4 u[8];
#pragma unroll
      for (int k = 0; k < 8; ++k) u[k] = ld_sc(rws, WS_X1 + (h * 16 + 1) * 1024 + lane * 16, k * 2048);
      bool ok = true;
#pragma unroll
      for (int k = 0; k < 8; ++k) ok = ok && u[k][1] == tag && u[k][3] == tag;
      a0x = (__uint_as_float(u[0][0]) + __uint_as_float(u[2][0])) + (__uint_as_float(u[4][0]) + __uint_as_float(u[6][0]));
      a0y = (__uint_as_float(u[0][2]) + __uint_as_float(u[2][2])) + (__uint_as_float(u[4][2]) + __uint_as_float(u[6][2]));
      a1x = (__uint_as_float(u[1][0]) + __uint_as_float(u[3][0])) + (__uint_as_float(u[5][0]) + __uint_as_float(u[7][0]));
      a1y = (__uint_as_float(u[1][2]) + __uint_as_float(u[3][2])) + (__uint_as_float(u[5][2]) + __uint_as_float(u[7][2]));
      if (__all(ok) || sp >= SPIN_CAP) break;
    }
    STAMP(4);
    const float g0 = rbf(a0x), g1 = rbf(a0y), u0 = rbf(a1x), u1 = rbf(a1y);
    const float s0 = rbf(__builtin_amdgcn_rcpf(1.f + __builtin_amdgcn_exp2f(-g0 * 1.4426950408889634f)));
    const float s1 = rbf(__builtin_amdgcn_rcpf(1.f + __builtin_amdgcn_exp2f(-g1 * 1.4426950408889634f)));
    const u32 hp = (lane & 31) < 4 * nch ? pk2(s0 * u0, s1 * u1) : 0u;
    lds[L_HPK + lane] = hp;
    CBAR();
    const u32x4 Ah = *(LAS u32x4*)(lds + L_HPK + ab * 32 + 8 * gq + 4 * ahi);
    f32x4 acc[8];
    u32x4 Bf[8];
    tr8(W, Bf);
#pragma unroll
    for (int t = 0; t < 8; ++t) acc[t] = mfma(Ah, Bf[t], f32x4{0.f, 0.f, 0.f, 0.f});
    epi(acc, lane, e0, e1);
    *(LAS f32x2*)(lds + L_YF + 2 * lane) = f32x2{e0, e1};
    CBAR();
    if (lane == 0) *(volatile LAS u32*)(lds + L_FF) = 1u;
    STAMP(6);
    STFLUSH();
    return;
  }
  {
    float qa, qb;
    for (u32 sp = 0; lds_ld(lds + L_KQ) == 0u && sp < LSPIN_CAP; ++sp) __builtin_amdgcn_s_sleep(2);
    STAMP(14);
    for (u32 sp = 0;; ++sp) {
      CBAR();
      u32x4 u[4];
#pragma unroll
      for (int k = 0; k < 4; ++k) u[k] = ld_sc(rws, WS_X1 + (h * 16 + 2 * (lane >> 5)) * 1024 + ((j >> 2) * 32 + (lane & 31)) * 16, k * 4096);
      bool ok = true;
#pragma unroll
      for (int k = 0; k < 4; ++k) ok = ok && u[k][1] == tag && u[k][3] == tag;
      qa = (__uint_as_float(u[0][0]) + __uint_as_float(u[1][0])) + (__uint_as_float(u[2][0]) + __uint_as_float(u[3][0]));
      qb = (__uint_as_float(u[0][2]) + __uint_as_float(u[1][2])) + (__uint_as_float(u[2][2]) + __uint_as_float(u[3][2]));
      if (__all(ok) || sp >= SPIN_CAP) break;
    }
    lds[L_QPK + lane] = pk2(qa, qb);
    CBAR();
    if (lane == 0) *(volatile LAS u32*)(lds + L_QF) = 1u;
  }
  STAMP(4);
  for (u32 sp = 0;; ++sp) {
    CBAR();
    const u32x4 a = *(volatile LAS u32x4*)(lds + L_VI);
    if ((a[0] & a[1] & a[2] & a[3]) != 0u || sp >= LSPIN_CAP) break;
    __builtin_amdgcn_s_sleep(2);
  }
  {
    const rsrc_t rb = mk_rsrc(wo + (size_t)(h * 128) * D + 64 * j, 128u * 1024u);
    const u32 vo = rho * 8192 + chunk * 16;
#pragma unroll
    for (int t = 0; t < 16; ++t) { W[t] = ld_nt(rb, vo, (t >> 3) * 65536 + (t & 7) * 1024); SCHEDB(); }
  }
  STAMP(5);
  float Mcu;
  for (u32 sp = 0;; ++sp) {
    CBAR();
    const u32x4 a = *(volatile LAS u32x4*)(lds + L_PF);
    if ((a[0] & a[1] & a[2] & a[3]) != 0u || sp >= LSPIN_CAP) break;
  }
  CBAR();
  {
    const u32x4 a = *(LAS u32x4*)(lds + L_MX), bq = *(LAS u32x4*)(lds + L_MX + 4), lq = *(LAS u32x4*)(lds + L_PL);
    Mcu = fmaxf(fmaxf(__uint_as_float(a[0]), __uint_as_float(a[2])), fmaxf(__uint_as_float(bq[0]), __uint_as_float(bq[2])));
    const float Lcu = (__uint_as_float(lq[0]) + __uint_as_float(lq[1])) + (__uint_as_float(lq[2]) + __uint_as_float(lq[3]));
    const f32x2 p0 = *(LAS f32x2*)(lds + L_PART + 2 * lane), p1 = *(LAS f32x2*)(lds + L_PART + 128 + 2 * lane),
                p2 = *(LAS f32x2*)(lds + L_PART + 256 + 2 * lane), p3 = *(LAS f32x2*)(lds + L_PART + 384 + 2 * lane);
    const u32 xb = WS_X2 + (h * 8 + j) * 2048;
    st_unit(rws, xb + lane * 16, (p0[0] + p1[0]) + (p2[0] + p3[0]), (p0[1] + p1[1]) + (p2[1] + p3[1]), tag);
    if (lane == 0) st_unit(rws, xb + 1024, Mcu, Lcu, tag);
  }
  STAMP(7);
  u32x4 u[8], us;
  us = ld_sc(rws, WS_X2 + (h * 8 + (lane & 7)) * 2048 + 1024, 0);
#pragma unroll
  for (int k = 0; k < 8; ++k) u[k] = ld_sc(rws, WS_X2 + h * 8 * 2048 + lane * 16, k * 2048);
  SCHEDB();
  u32x4 B0[8], B1[8];
  tr8(W, B0); tr8(W + 8, B1);
#pragma unroll
  for (int t = 0; t < 8; ++t) asm volatile("" : "+v"(B0[t]), "+v"(B1[t]));
  STAMP(6);
  for (u32 sp = 0;; ++sp) {
    bool ok = us[1] == tag && us[3] == tag;
#pragma unroll
    for (int k = 0; k < 8; ++k) ok = ok && u[k][1] == tag && u[k][3] == tag;
    if (__all(ok) || sp >= SPIN_CAP) break;
    CBAR();
    us = ld_sc(rws, WS_X2 + (h * 8 + (lane & 7)) * 2048 + 1024, 0);
#pragma unroll
    for (int k = 0; k < 8; ++k) u[k] = ld_sc(rws, WS_X2 + h * 8 * 2048 + lane * 16, k * 2048);
  }
  STAMP(8);
  {
    const float m = __uint_as_float(us[0]), l = __uint_as_float(us[2]);
    float Mb = fmaxf(m, dpp<XOR1>(m)); Mb = fmaxf(Mb, dpp<XOR2>(Mb));
    const float w = __builtin_amdgcn_exp2f((m - Mb) * 1.4426950408889634f);
    float L = w * l; L += dpp<XOR1>(L); L += dpp<XOR2>(L);
    const float coef = w * __builtin_amdgcn_rcpf(L);
    float o00 = 0.f, o01 = 0.f, o10 = 0.f, o11 = 0.f;
#pragma unroll
    for (int k = 0; k < 4; ++k) {
      const float ca = __uint_as_float(__builtin_amdgcn_readlane(__float_as_uint(coef), k));
      const float cb = __uint_as_float(__builtin_amdgcn_readlane(__float_as_uint(coef), 4 + k));
      o00 = fmaf(ca, __uint_as_float(u[k][0]), o00); o01 = fmaf(ca, __uint_as_float(u[k][2]), o01);
      o10 = fmaf(cb, __uint_as_float(u[4 + k][0]), o10); o11 = fmaf(cb, __uint_as_float(u[4 + k][2]), o11);
    }
    lds[L_OPK + lane] = pk2(o00, o01);
    lds[L_OPK + 64 + lane] = pk2(o10, o11);
  }
  CBAR();
  {
    const u32x4 Ao0 = *(LAS u32x4*)(lds + L_OPK + ab * 64 + 8 * gq + 4 * ahi);
    const u32x4 Ao1 = *(LAS u32x4*)(lds + L_OPK + ab * 64 + 32 + 8 * gq + 4 * ahi);
    f32x4 acc[8];
#pragma unroll
    for (int t = 0; t < 8; ++t) acc[t] = mfma(Ao0, B0[t], f32x4{0.f, 0.f, 0.f, 0.f});
#pragma unroll
    for (int t = 0; t < 8; ++t) acc[t] = mfma(Ao1, B1[t], acc[t]);
    epi(acc, lane, e0, e1);
  }
  for (u32 sp = 0; lds_ld(lds + L_FF) == 0u && sp < LSPIN_CAP; ++sp) __builtin_amdgcn_s_sleep(1);
  CBAR();
  {
    const f32x2 yf = *(LAS f32x2*)(lds + L_YF + 2 * lane);
    const u32 cp = 4 * (lane & 7) + (lane >> 4), bb = (lane >> 3) & 1;
    st_unit(rws, WS_X3 + ((j * 32 + cp) * 64 + 2 * h + bb) * 16, e0 + yf[0], e1 + yf[1], tag);
  }
  STAMP(9);
  float y0, y1;
  for (u32 sp = 0;; ++sp) {
    CBAR();
    const u32x4 v = ld_sc(rws, WS_X3 + (j * 32 + h) * 1024 + lane * 16, 0);
    y0 = __uint_as_float(v[0]); y1 = __uint_as_float(v[2]);
    if (__all(v[1] == tag && v[3] == tag) || sp >= SPIN_CAP) break;
  }
  STAMP(10);
  y0 += dpp<XOR2>(y0); y1 += dpp<XOR2>(y1);
  y0 += dpp<ROR4>(y0); y1 += dpp<ROR4>(y1);
  y0 += dpp<ROR8>(y0); y1 += dpp<ROR8>(y1);
  y0 = xrow_sum(y0); y1 = xrow_sum(y1);
  if (lane < 2) {
    const u32 o = pk2(bflo(xfin) + y0, bfhi(xfin) + y1);
    *(GAS u32*)(out + lane * D + 64 * j + 2 * h) = o;
  }
  if (g == 0 && lane == 0) __builtin_amdgcn_raw_buffer_store_b32(tag, rws, WS_E, 0, AUX_SC1);
  STAMP(11);
  STFLUSH();
}

extern "C" void kernel_launch(void* const* d_in, const int* in_sizes, int n_in,
                              void* d_out, int out_size, void* d_ws, size_t ws_size,
                              hipStream_t stream) {
  constexpr int LDS_BYTES = 84 * 1024;
  (void)hipFuncSetAttribute((const void*)fused_decode, hipFuncAttributeMaxDynamicSharedMemorySize, LDS_BYTES);
  fused_decode<<<256, NTHR, LDS_BYTES, stream>>>((const u16*)d_in[0], (const u16*)d_in[1], (const u16*)d_in[2], (const u16*)d_in[3],
      (const u16*)d_in[4], (const u16*)d_in[6], (const u16*)d_in[7], (const u16*)d_in[8], (u16*)d_out, (unsigned char*)d_ws);
}
```

```cpp
#define NO_CPF
#include <hip/hip_runtime.h>
#include <hip/hip_bf16.h>
#include <stdint.h>

typedef unsigned short u16;
typedef unsigned u32;
typedef u32 u32x4 __attribute__((ext_vector_type(4)));
typedef u32 u32x2 __attribute__((ext_vector_type(2)));
typedef float f32x4 __attribute__((ext_vector_type(4)));
typedef float f32x2 __attribute__((ext_vector_type(2)));
typedef __bf16 bf16x2 __attribute__((ext_vector_type(2)));
typedef __bf16 bf16x8 __attribute__((ext_vector_type(8)));
#define GAS __attribute__((address_space(1)))
#define LAS __attribute__((address_space(3)))

constexpr int D = 512, NH = 32, DH = 128, SK = 4096, WIN = 1024, FF = 1376;
constexpr int CODE_BYTES = 12 * 1024;
constexpr int NTHR = 640;
#define KWIN 6
#define VWIN 6
#define KSIG 3
#define KQT 15
#define X2D 12
#define X3D 12
#define FWIN 4
constexpr int AUX_NT = 2, AUX_SC1 = 16;
constexpr u32 SPIN_CAP = 1u << 13;
constexpr u32 LSPIN_CAP = 1u << 17;

constexpr u32 WS_E = 0;
constexpr u32 WS_X1 = 4096;
constexpr u32 WS_X2 = WS_X1 + 32 * 8 * 2 * 1024;
constexpr u32 WS_X3 = WS_X2 + 32 * 8 * 2048;
constexpr u32 WS_END = WS_X3 + 8 * 32 * 1024;

constexpr int L_QF = 0;
constexpr int L_FF = 1;
constexpr int L_KQ = 2;
constexpr int L_PC = 3;
constexpr int L_X2 = 4;
constexpr int L_TAG = 5;
constexpr int L_MX = 8;
constexpr int L_KI = 16;
constexpr int L_PR = 20;
constexpr int L_VI = 24;
constexpr int L_QPK = 32;
constexpr int L_HPK = 96;
constexpr int L_OPK = 160;
constexpr int L_YF = 288;
constexpr int L_PL = 416;
constexpr int L_P = 432;
constexpr int L_PART = 688;
constexpr int L_DW = L_PART + 512;

__device__ __forceinline__ float bflo(u32 v) { return __uint_as_float(v << 16); }
__device__ __forceinline__ float bfhi(u32 v) { return __uint_as_float(v & 0xffff0000u); }
__device__ __forceinline__ u32 pk2(float lo, float hi) {
  bf16x2 r = {(__bf16)lo, (__bf16)hi};
  return __builtin_bit_cast(u32, r);
}
__device__ __forceinline__ float rbf(float f) { return bflo(pk2(f, f)); }
template <int CTRL> __device__ __forceinline__ float dpp(float x) {
  return __builtin_bit_cast(float, __builtin_amdgcn_mov_dpp(__builtin_bit_cast(int, x), CTRL, 0xf, 0xf, true));
}
constexpr int XOR1 = 0xB1, XOR2 = 0x4E, XOR7 = 0x141, ROR8 = 0x128, ROR4 = 0x124;
__device__ __forceinline__ float xrow_sum(float x) {
  auto s = __builtin_amdgcn_permlane16_swap(__float_as_uint(x), __float_as_uint(x), false, false);
  x = __uint_as_float(s[0]) + __uint_as_float(s[1]);
  auto t = __builtin_amdgcn_permlane32_swap(__float_as_uint(x), __float_as_uint(x), false, false);
  return __uint_as_float(t[0]) + __uint_as_float(t[1]);
}
__device__ __forceinline__ float xrow_max(float x) {
  auto s = __builtin_amdgcn_permlane16_swap(__float_as_uint(x), __float_as_uint(x), false, false);
  x = fmaxf(__uint_as_float(s[0]), __uint_as_float(s[1]));
  auto t = __builtin_amdgcn_permlane32_swap(__float_as_uint(x), __float_as_uint(x), false, false);
  return fmaxf(__uint_as_float(t[0]), __uint_as_float(t[1]));
}
__device__ __forceinline__ float wave_sum(float x) {
  x += dpp<XOR1>(x); x += dpp<XOR2>(x); x += dpp<XOR7>(x); x += dpp<ROR8>(x);
  return xrow_sum(x);
}
__device__ __forceinline__ float wave_max(float x) {
  x = fmaxf(x, dpp<XOR1>(x)); x = fmaxf(x, dpp<XOR2>(x)); x = fmaxf(x, dpp<XOR7>(x)); x = fmaxf(x, dpp<ROR8>(x));
  return xrow_max(x);
}
__device__ __forceinline__ float dot2(u32 a, u32 b, float c) {
  return __builtin_amdgcn_fdot2_f32_bf16(__builtin_bit_cast(bf16x2, a), __builtin_bit_cast(bf16x2, b), c, false);
}
typedef __amdgpu_buffer_rsrc_t rsrc_t;
__device__ __forceinline__ rsrc_t mk_rsrc(const void* p, u32 bytes) {
  return __builtin_amdgcn_make_buffer_rsrc((void*)p, 0, bytes, 0x00020000);
}
__device__ __forceinline__ u32x4 ld_nt(rsrc_t r, u32 voff, u32 soff) { return __builtin_amdgcn_raw_buffer_load_b128(r, voff, soff, AUX_NT); }
__device__ __forceinline__ u32x4 ld_pl(rsrc_t r, u32 voff, u32 soff) { return __builtin_amdgcn_raw_buffer_load_b128(r, voff, soff, 0); }
__device__ __forceinline__ u32x4 ld_sc(rsrc_t r, u32 voff, u32 soff) { return __builtin_amdgcn_raw_buffer_load_b128(r, voff, soff, AUX_SC1); }
__device__ __forceinline__ void st_unit(rsrc_t r, u32 voff, float a, float b, u32 tag) {
  u32x4 v = {__float_as_uint(a), tag, __float_as_uint(b), tag};
  __builtin_amdgcn_raw_buffer_store_b128(v, r, voff, 0, AUX_SC1);
}
__device__ __forceinline__ f32x4 mfma(u32x4 a, u32x4 b, f32x4 c) {
  return __builtin_amdgcn_mfma_f32_16x16x32_bf16(__builtin_bit_cast(bf16x8, a), __builtin_bit_cast(bf16x8, b), c, 0, 0, 0);
}
template <int N> __device__ __forceinline__ void waitv() { asm volatile("s_waitcnt vmcnt(%0)" :: "i"(N) : "memory"); }
#define CBAR() asm volatile("" ::: "memory")
#define SCHEDB() __builtin_amdgcn_sched_barrier(0)
__device__ __forceinline__ u32 lds_ld(volatile LAS u32* p) { return *p; }

#define STAMP(i) do {} while (0)
#define STFLUSH() do {} while (0)
#define STDECL() do {} while (0)

__device__ __forceinline__ void tr8(const u32x4* W, u32x4* Bf) {
#pragma unroll
  for (int q = 0; q < 4; ++q) {
#pragma unroll
    for (int p = 0; p < 4; ++p) {
      Bf[2 * q][p] = __builtin_amdgcn_perm(W[2 * p + 1][q], W[2 * p][q], 0x05040100u);
      Bf[2 * q + 1][p] = __builtin_amdgcn_perm(W[2 * p + 1][q], W[2 * p][q], 0x07060302u);
    }
  }
}
__device__ __forceinline__ void epi(const f32x4* acc, int lane, float& e0, float& e1) {
  const bool lo = (lane & 8) == 0;
  float v[8];
#pragma unroll
  for (int j = 0; j < 8; ++j) {
    const float send = lo ? acc[j][2] : acc[j][1];
    const float keep = lo ? acc[j][0] : acc[j][3];
    v[j] = keep + dpp<ROR8>(send);
  }
  const int grp = lane >> 4;
  e0 = grp == 0 ? v[0] : grp == 1 ? v[2] : grp == 2 ? v[4] : v[6];
  e1 = grp == 0 ? v[1] : grp == 1 ? v[3] : grp == 2 ? v[5] : v[7];
}

__global__ void __launch_bounds__(NTHR, 3)
fused_decode(const u16* __restrict__ x, const u16* __restrict__ kc, const u16* __restrict__ vc,
             const u16* __restrict__ wq, const u16* __restrict__ wo, const u16* __restrict__ wg,
             const u16* __restrict__ wu, const u16* __restrict__ wd, u16* __restrict__ out, unsigned char* __restrict__ ws) {
  extern __shared__ __attribute__((aligned(16))) u32 lds_raw[];
  LAS u32* lds = (LAS u32*)lds_raw;
  const int tid = threadIdx.x, lane = tid & 63;
  const int wave = __builtin_amdgcn_readfirstlane(tid >> 6);
  STDECL();
  lds[tid & 31] = 0u;
  asm volatile("s_waitcnt lgkmcnt(0)" :: "s"(x), "s"(kc), "s"(vc), "s"(wq), "s"(wo), "s"(wg), "s"(wu), "s"(wd), "s"(out), "s"(ws) : "memory");
  STAMP(0);
  const int g = blockIdx.x;
  const int h = (g & 7) * 4 + (g >> 6), j = (g >> 3) & 7;

  if (wave >= 2) {
    const int sw = (wave - 2) & 3;
    const int b = j >> 2, s = j & 3;
    const size_t kvoff = ((size_t)(b * NH + h) * SK + (SK - WIN) + 256 * s + 64 * sw) * DH;
    const u32 vo = lane * 16;
    const int c = lane & 15;
#define KVOFF(t) ((t) * 1024)
    if (wave < 6) {
      const rsrc_t rk = mk_rsrc(kc + kvoff, 16384);
      u32x4 K[16];
      asm volatile("s_barrier" ::: "memory");
      SCHEDB();
#pragma unroll
      for (int t = 0; t < 16; ++t) {
        if (t >= KWIN) waitv<KWIN - 1>();
        SCHEDB();
        K[t] = ld_nt(rk, vo, KVOFF(t));
        SCHEDB();
        if (t == KQT) { if (sw == 0 && lane == 0) *(volatile LAS u32*)(lds + L_KQ) = 1u; SCHEDB(); }
      }
      waitv<KSIG>();
      if (lane == 0) *(volatile LAS u32*)(lds + L_KI + sw) = 1u;
      STAMP(1);
      for (u32 sp = 0; lds_ld(lds + L_QF) == 0u && sp < LSPIN_CAP; ++sp) __builtin_amdgcn_s_sleep(1);
      CBAR();
      STAMP(2);
      const u32x4 q = *(LAS u32x4*)(lds + L_QPK + 4 * c);
      float xs[16];
#pragma unroll
      for (int t = 0; t < 16; ++t) {
        float d = dot2(K[t][0], q[0], 0.f);
        d = dot2(K[t][1], q[1], d); d = dot2(K[t][2], q[2], d); xs[t] = dot2(K[t][3], q[3], d);
      }
#define TR_STEP(HALF, CTRL, BIT) _Pragma("unroll") for (int u = 0; u < (HALF); ++u) { \
        const bool up_ = (c & (BIT)) != 0; const float keep_ = up_ ? xs[u + (HALF)] : xs[u], send_ = up_ ? xs[u] : xs[u + (HALF)]; \
        xs[u] = keep_ + dpp<CTRL>(send_); }
      TR_STEP(8, ROR8, 8) TR_STEP(4, XOR7, 4) TR_STEP(2, XOR2, 2) TR_STEP(1, XOR1, 1)
#undef TR_STEP
      const float sc = rbf(rbf(xs[0]) * 0.08838834764831845f);
      const float mw = wave_max(sc);
      if (lane == 0) { *(LAS u32x2*)(lds + L_MX + 2 * sw) = u32x2{__float_as_uint(mw), 1u}; }
      STAMP(3);
      float M;
      for (u32 sp = 0;; ++sp) {
        CBAR();
        const u32x4 a = *(volatile LAS u32x4*)(lds + L_MX), bq = *(volatile LAS u32x4*)(lds + L_MX + 4);
        M = fmaxf(fmaxf(__uint_as_float(a[0]), __uint_as_float(a[2])), fmaxf(__uint_as_float(bq[0]), __uint_as_float(bq[2])));
        if ((a[1] & a[3] & bq[1] & bq[3]) != 0u || sp >= LSPIN_CAP) break;
      }
      const float p = __builtin_amdgcn_exp2f((sc - M) * 1.4426950408889634f);
      lds[L_P + 64 * sw + lane] = __float_as_uint(p);
      const float lw = wave_sum(p);
      if (lane == 0) lds[L_PL + sw] = __float_as_uint(lw);
      CBAR();
      if (lane == 0) *(volatile LAS u32*)(lds + L_PR + sw) = 1u;
      STAMP(4);
      STFLUSH();
      return;
    }
    const rsrc_t rv = mk_rsrc(vc + kvoff, 16384);
    u32x4 V[16];
    asm volatile("s_barrier" ::: "memory");
    for (u32 sp = 0; lds_ld(lds + L_KI + sw) == 0u && sp < LSPIN_CAP; ++sp) __builtin_amdgcn_s_sleep(2);
    SCHEDB();
    STAMP(1);
#pragma unroll
    for (int t = 0; t < 16; ++t) {
      if (t >= VWIN) waitv<VWIN - 1>();
      SCHEDB();
      V[t] = ld_nt(rv, vo, KVOFF(t));
      SCHEDB();
    }
    if (lane == 0) *(volatile LAS u32*)(lds + L_VI + sw) = 1u;
    STAMP(2);
    for (u32 sp = 0; lds_ld(lds + L_PR + sw) == 0u && sp < LSPIN_CAP; ++sp) __builtin_amdgcn_s_sleep(1);
    CBAR();
    const float p = __uint_as_float(lds[L_P + 64 * sw + lane]);
    STAMP(3);
    float acc[8];
#pragma unroll
    for (int k = 0; k < 8; ++k) acc[k] = 0.f;
#define PV_STEP(T) { const float pb = dpp<0x150 + (T)>(p); \
      _Pragma("unroll") for (int k = 0; k < 4; ++k) { acc[2 * k] = fmaf(pb, bflo(V[T][k]), acc[2 * k]); acc[2 * k + 1] = fmaf(pb, bfhi(V[T][k]), acc[2 * k + 1]); } }
    PV_STEP(0) PV_STEP(1) PV_STEP(2) PV_STEP(3) PV_STEP(4) PV_STEP(5) PV_STEP(6) PV_STEP(7)
    PV_STEP(8) PV_STEP(9) PV_STEP(10) PV_STEP(11) PV_STEP(12) PV_STEP(13) PV_STEP(14) PV_STEP(15)
#undef PV_STEP
    float v1[4];
#pragma unroll
    for (int k = 0; k < 4; ++k) {
      auto sw2 = __builtin_amdgcn_permlane32_swap(__float_as_uint(acc[k]), __float_as_uint(acc[k + 4]), false, false);
      v1[k] = __uint_as_float(sw2[0]) + __uint_as_float(sw2[1]);
    }
    auto s0 = __builtin_amdgcn_permlane16_swap(__float_as_uint(v1[0]), __float_as_uint(v1[2]), false, false);
    auto s1 = __builtin_amdgcn_permlane16_swap(__float_as_uint(v1[1]), __float_as_uint(v1[3]), false, false);
    const float o0 = __uint_as_float(s0[0]) + __uint_as_float(s0[1]), o1 = __uint_as_float(s1[0]) + __uint_as_float(s1[1]);
    *(LAS f32x2*)(lds + L_PART + 128 * sw + 8 * c + 2 * (lane >> 4)) = f32x2{o0, o1};
    CBAR();
    u32 arr = 0;
    if (lane == 0) arr = __hip_atomic_fetch_add(lds + L_PC, 1u, __ATOMIC_RELAXED, __HIP_MEMORY_SCOPE_WORKGROUP);
    arr = __builtin_amdgcn_readfirstlane(arr);
    if (arr == 3u) {
      CBAR();
      const f32x2 p0 = *(LAS f32x2*)(lds + L_PART + 2 * lane), p1 = *(LAS f32x2*)(lds + L_PART + 128 + 2 * lane),
                  p2 = *(LAS f32x2*)(lds + L_PART + 256 + 2 * lane), p3 = *(LAS f32x2*)(lds + L_PART + 384 + 2 * lane);
      const u32x4 a = *(LAS u32x4*)(lds + L_MX), bq = *(LAS u32x4*)(lds + L_MX + 4), lq = *(LAS u32x4*)(lds + L_PL);
      const u32 tag = lds[L_TAG];
      const rsrc_t rws = mk_rsrc(ws, WS_END);
      const u32 xb = WS_X2 + (h * 8 + j) * 2048;
      st_unit(rws, xb + lane * 16, (p0[0] + p1[0]) + (p2[0] + p3[0]), (p0[1] + p1[1]) + (p2[1] + p3[1]), tag);
      const float Mcu = fmaxf(fmaxf(__uint_as_float(a[0]), __uint_as_float(a[2])), fmaxf(__uint_as_float(bq[0]), __uint_as_float(bq[2])));
      const float Lcu = (__uint_as_float(lq[0]) + __uint_as_float(lq[1])) + (__uint_as_float(lq[2]) + __uint_as_float(lq[3]));
      if (lane == 0) { st_unit(rws, xb + 1024, Mcu, Lcu, tag); *(volatile LAS u32*)(lds + L_X2) = 1u; }
    }
    STAMP(4);
    STFLUSH();
    return;
  }

  __builtin_amdgcn_s_setprio(3);
  const int f = wave;
  const rsrc_t rws = mk_rsrc(ws, WS_END);
  const int m4 = lane & 3, gq = lane >> 4, rho = lane >> 3, chunk = lane & 7;
  const int ab = (m4 >> 1), ahi = m4 & 1;
  const int kq = j >> 1;
  const int nch = h < 12 ? 6 : 5, c0 = h < 12 ? 6 * h : 72 + 5 * (h - 12);
  const rsrc_t rx = mk_rsrc(x, 2048);
  const u32x4 xs0 = ld_pl(rx, lane * 16, 0), xs1 = ld_pl(rx, lane * 16, 1024);
  const u32 xaoff = ab * 1024 + (128 * kq + 16 * gq + 8 * ahi) * 2;
  const u32x4 xa0 = ld_pl(rx, xaoff, 0), xa1 = ld_pl(rx, xaoff, 128);
  const u32 E = __builtin_amdgcn_raw_buffer_load_b32(rws, WS_E, 0, AUX_SC1);
  const u32 xfin = __builtin_amdgcn_raw_buffer_load_b32(rx, (lane & 1) * 1024 + (64 * j + 2 * h) * 2, 0, 0);
  u32x4 W[16];
  {
    const u16* pa = f == 0 ? wq + (size_t)(128 * kq) * 4096 + h * 128 + 64 * (j & 1)
                           : ((j & 1) ? wu : wg) + (size_t)(128 * kq) * FF + 8 * c0;
    const u32 strb = f == 0 ? 8192u : 2752u;
    const int ch = f == 0 ? chunk : (chunk < nch ? chunk : nch - 1);
    const rsrc_t ra = mk_rsrc(pa, 128u * strb);
    const u32 vo = rho * 8 * strb + ch * 16;
#pragma unroll
    for (int t = 0; t < 16; ++t) {
      W[t] = ld_nt(ra, vo, ((t >> 3) * 64 + (t & 7)) * strb);
      SCHEDB(); }
    asm volatile("s_barrier" ::: "memory");
  }
  SCHEDB();
  STAMP(1);
  float mb, rd;
  {
    float s0 = 0.f, s1 = 0.f;
#pragma unroll
    for (int k = 0; k < 4; ++k) { s0 += bflo(xs0[k]) + bfhi(xs0[k]); s1 += bflo(xs1[k]) + bfhi(xs1[k]); }
    const float mean0 = wave_sum(s0) * (1.f / 512.f), mean1 = wave_sum(s1) * (1.f / 512.f);
    float q0 = 0.f, q1 = 0.f;
#pragma unroll
    for (int k = 0; k < 4; ++k) {
      float a = bflo(xs0[k]) - mean0, bb = bfhi(xs0[k]) - mean0; q0 = fmaf(a, a, q0); q0 = fmaf(bb, bb, q0);
      a = bflo(xs1[k]) - mean1; bb = bfhi(xs1[k]) - mean1; q1 = fmaf(a, a, q1); q1 = fmaf(bb, bb, q1);
    }
    const float var0 = wave_sum(q0) * (1.f / 512.f), var1 = wave_sum(q1) * (1.f / 512.f);
    const float mean = ab ? mean1 : mean0, var = ab ? var1 : var0;
    mb = rbf(mean);
    rd = rbf(__builtin_amdgcn_sqrtf(rbf(rbf(var) + 1e-5f)));
  }
  const float rinv = __builtin_amdgcn_rcpf(rd);
  u32x4 A0, A1;
#pragma unroll
  for (int k = 0; k < 4; ++k) {
    float v[4] = {rbf(bflo(xa0[k]) - mb), rbf(bfhi(xa0[k]) - mb), rbf(bflo(xa1[k]) - mb), rbf(bfhi(xa1[k]) - mb)};
#pragma unroll
    for (int e = 0; e < 4; ++e) { const float q0 = v[e] * rinv; const float r = fmaf(-q0, rd, v[e]); v[e] = fmaf(r, rinv, q0); }
    A0[k] = pk2(v[0], v[1]); A1[k] = pk2(v[2], v[3]);
  }
  STAMP(2);
  const u32 tag = E + 1u;
  float e0, e1;
  {
    f32x4 acc[8];
    u32x4 Bf[8];
    tr8(W, Bf);
#pragma unroll
    for (int t = 0; t < 8; ++t) acc[t] = mfma(A0, Bf[t], f32x4{0.f, 0.f, 0.f, 0.f});
    tr8(W + 8, Bf);
#pragma unroll
    for (int t = 0; t < 8; ++t) acc[t] = mfma(A1, Bf[t], acc[t]);
    epi(acc, lane, e0, e1);
  }
  STAMP(13);
  const u32 uidx = ((lane >> 3) & 1) * 32 + 4 * (lane & 7) + (lane >> 4);
  st_unit(rws, WS_X1 + ((h * 8 + j) * 2 + f) * 1024 + uidx * 16, e0, e1, tag);
  STAMP(3);
  if (f == 1) {
    float a0x, a0y, a1x, a1y;
    for (u32 sp = 0; lds_ld(lds + L_KI) == 0u && sp < LSPIN_CAP; ++sp) __builtin_amdgcn_s_sleep(2);
    for (u32 sp = 0;; ++sp) {
      CBAR();
      u32x4 u[8];
#pragma unroll
      for (int k = 0; k < 8; ++k) u[k] = ld_sc(rws, WS_X1 + (h * 16 + 1) * 1024 + lane * 16, k * 2048);
      bool ok = true;
#pragma unroll
      for (int k = 0; k < 8; ++k) ok = ok && u[k][1] == tag && u[k][3] == tag;
      a0x = (__uint_as_float(u[0][0]) + __uint_as_float(u[2][0])) + (__uint_as_float(u[4][0]) + __uint_as_float(u[6][0]));
      a0y = (__uint_as_float(u[0][2]) + __uint_as_float(u[2][2])) + (__uint_as_float(u[4][2]) + __uint_as_float(u[6][2]));
      a1x = (__uint_as_float(u[1][0]) + __uint_as_float(u[3][0])) + (__uint_as_float(u[5][0]) + __uint_as_float(u[7][0]));
      a1y = (__uint_as_float(u[1][2]) + __uint_as_float(u[3][2])) + (__uint_as_float(u[5][2]) + __uint_as_float(u[7][2]));
      if (__all(ok) || sp >= SPIN_CAP) break;
    }
    STAMP(4);
    const float g0 = rbf(a0x), g1 = rbf(a0y), u0 = rbf(a1x), u1 = rbf(a1y);
    const float s0 = rbf(__builtin_amdgcn_rcpf(1.f + __builtin_amdgcn_exp2f(-g0 * 1.4426950408889634f)));
    const float s1 = rbf(__builtin_amdgcn_rcpf(1.f + __builtin_amdgcn_exp2f(-g1 * 1.4426950408889634f)));
    const u32 hp = (lane & 31) < 4 * nch ? pk2(s0 * u0, s1 * u1) : 0u;
    lds[L_HPK + lane] = hp;
    for (u32 sp = 0;; ++sp) {
      CBAR();
      const u32x4 a = *(volatile LAS u32x4*)(lds + L_VI);
      if ((a[0] & a[1] & a[2] & a[3]) != 0u || sp >= LSPIN_CAP) break;
      __builtin_amdgcn_s_sleep(2);
    }
    {
      const u16* pb = wd + (size_t)(8 * c0) * D + 64 * j;
      const int rr = rho < nch ? rho : nch - 1;
      const rsrc_t rb = mk_rsrc(pb, 64u * 1024u);
      const u32 vo = rr * 8192 + chunk * 16;
#pragma unroll
      for (int t = 0; t < 8; ++t) { W[t] = ld_nt(rb, vo, t * 1024); SCHEDB(); }
    }
    CBAR();
    const u32x4 Ah = *(LAS u32x4*)(lds + L_HPK + ab * 32 + 8 * gq + 4 * ahi);
    f32x4 acc[8];
    u32x4 Bf[8];
    tr8(W, Bf);
#pragma unroll
    for (int t = 0; t < 8; ++t) acc[t] = mfma(Ah, Bf[t], f32x4{0.f, 0.f, 0.f, 0.f});
    epi(acc, lane, e0, e1);
    *(LAS f32x2*)(lds + L_YF + 2 * lane) = f32x2{e0, e1};
    CBAR();
    if (lane == 0) *(volatile LAS u32*)(lds + L_FF) = 1u;
    STAMP(6);
    STFLUSH();
    return;
  }
  {
    float qa, qb;
    for (u32 sp = 0; lds_ld(lds + L_KQ) == 0u && sp < LSPIN_CAP; ++sp) __builtin_amdgcn_s_sleep(2);
    STAMP(14);
    for (u32 sp = 0;; ++sp) {
      CBAR();
      u32x4 u[4];
#pragma unroll
      for (int k = 0; k < 4; ++k) u[k] = ld_sc(rws, WS_X1 + (h * 16 + 2 * (lane >> 5)) * 1024 + ((j >> 2) * 32 + (lane & 31)) * 16, k * 4096);
      bool ok = true;
#pragma unroll
      for (int k = 0; k < 4; ++k) ok = ok && u[k][1] == tag && u[k][3] == tag;
      qa = (__uint_as_float(u[0][0]) + __uint_as_float(u[1][0])) + (__uint_as_float(u[2][0]) + __uint_as_float(u[3][0]));
      qb = (__uint_as_float(u[0][2]) + __uint_as_float(u[1][2])) + (__uint_as_float(u[2][2]) + __uint_as_float(u[3][2]));
      if (__all(ok) || sp >= SPIN_CAP) break;
    }
    lds[L_QPK + lane] = pk2(qa, qb);
    if (lane == 0) lds[L_TAG] = tag;
    CBAR();
    if (lane == 0) *(volatile LAS u32*)(lds + L_QF) = 1u;
  }
  STAMP(4);
  for (u32 sp = 0;; ++sp) {
    CBAR();
    const u32x4 a = *(volatile LAS u32x4*)(lds + L_VI);
    if ((a[0] & a[1] & a[2] & a[3]) != 0u || sp >= LSPIN_CAP) break;
    __builtin_amdgcn_s_sleep(2);
  }
  {
    const rsrc_t rb = mk_rsrc(wo + (size_t)(h * 128) * D + 64 * j, 128u * 1024u);
    const u32 vo = rho * 8192 + chunk * 16;
#pragma unroll
    for (int t = 0; t < 16; ++t) { W[t] = ld_nt(rb, vo, (t >> 3) * 65536 + (t & 7) * 1024); SCHEDB(); }
  }
  STAMP(5);
  for (u32 sp = 0; lds_ld(lds + L_X2) == 0u && sp < LSPIN_CAP; ++sp) {}
  STAMP(7);
  __builtin_amdgcn_s_sleep(X2D);
  u32x4 u[8], us;
#define X2_ISSUE() do { us = ld_sc(rws, WS_X2 + (h * 8 + (lane & 7)) * 2048 + 1024, 0); \
    _Pragma("unroll") for (int k = 0; k < 8; ++k) u[k] = ld_sc(rws, WS_X2 + h * 8 * 2048 + lane * 16, k * 2048); SCHEDB(); } while (0)
  X2_ISSUE();
  u32x4 B0[8], B1[8];
  tr8(W, B0); tr8(W + 8, B1);
#pragma unroll
  for (int t = 0; t < 8; ++t) asm volatile("" : "+v"(B0[t]), "+v"(B1[t]));
  STAMP(6);
  float coef;
  for (u32 sp = 0;; ++sp) {
    const float m = __uint_as_float(us[0]), l = __uint_as_float(us[2]);
    float Mb = fmaxf(m, dpp<XOR1>(m)); Mb = fmaxf(Mb, dpp<XOR2>(Mb));
    const float w = __builtin_amdgcn_exp2f((m - Mb) * 1.4426950408889634f);
    float L = w * l; L += dpp<XOR1>(L); L += dpp<XOR2>(L);
    coef = w * __builtin_amdgcn_rcpf(L);
    asm volatile("" : "+v"(coef));
    SCHEDB();
    bool ok = us[1] == tag && us[3] == tag;
#pragma unroll
    for (int k = 0; k < 8; ++k) ok = ok && u[k][1] == tag && u[k][3] == tag;
    if (__all(ok) || sp >= SPIN_CAP) {
      break; }
    CBAR();
    X2_ISSUE();
  }
#undef X2_ISSUE
  STAMP(8);
  {
    float o00 = 0.f, o01 = 0.f, o10 = 0.f, o11 = 0.f;
#pragma unroll
    for (int k = 0; k < 4; ++k) {
      const float ca = __uint_as_float(__builtin_amdgcn_readlane(__float_as_uint(coef), k));
      const float cb = __uint_as_float(__builtin_amdgcn_readlane(__float_as_uint(coef), 4 + k));
      o00 = fmaf(ca, __uint_as_float(u[k][0]), o00); o01 = fmaf(ca, __uint_as_float(u[k][2]), o01);
      o10 = fmaf(cb, __uint_as_float(u[4 + k][0]), o10); o11 = fmaf(cb, __uint_as_float(u[4 + k][2]), o11);
    }
    lds[L_OPK + lane] = pk2(o00, o01);
    lds[L_OPK + 64 + lane] = pk2(o10, o11);
  }
  CBAR();
  {
    const u32x4 Ao0 = *(LAS u32x4*)(lds + L_OPK + ab * 64 + 8 * gq + 4 * ahi);
    const u32x4 Ao1 = *(LAS u32x4*)(lds + L_OPK + ab * 64 + 32 + 8 * gq + 4 * ahi);
    f32x4 acc[8];
#pragma unroll
    for (int t = 0; t < 8; ++t) acc[t] = mfma(Ao0, B0[t], f32x4{0.f, 0.f, 0.f, 0.f});
#pragma unroll
    for (int t = 0; t < 8; ++t) acc[t] = mfma(Ao1, B1[t], acc[t]);
    epi(acc, lane, e0, e1);
  }
  for (u32 sp = 0; lds_ld(lds + L_FF) == 0u && sp < LSPIN_CAP; ++sp) __builtin_amdgcn_s_sleep(1);
  CBAR();
  {
    const f32x2 yf = *(LAS f32x2*)(lds + L_YF + 2 * lane);
    const u32 cp = 4 * (lane & 7) + (lane >> 4), bb = (lane >> 3) & 1;
    st_unit(rws, WS_X3 + ((j * 32 + cp) * 64 + 2 * h + bb) * 16, e0 + yf[0], e1 + yf[1], tag);
  }
  STAMP(9);
  __builtin_amdgcn_s_sleep(X3D);
  float y0, y1;
  for (u32 sp = 0;; ++sp) {
    CBAR();
    const u32x4 v = ld_sc(rws, WS_X3 + (j * 32 + h) * 1024 + lane * 16, 0);
    y0 = __uint_as_float(v[0]); y1 = __uint_as_float(v[2]);
    if (__all(v[1] == tag && v[3] == tag) || sp >= SPIN_CAP) {
      break; }
  }
  STAMP(10);
  y0 += dpp<XOR2>(y0); y1 += dpp<XOR2>(y1);
  y0 += dpp<ROR4>(y0); y1 += dpp<ROR4>(y1);
  y0 += dpp<ROR8>(y0); y1 += dpp<ROR8>(y1);
  y0 = xrow_sum(y0); y1 = xrow_sum(y1);
  if (lane < 2) {
    const u32 o = pk2(bflo(xfin) + y0, bfhi(xfin) + y1);
    *(GAS u32*)(out + lane * D + 64 * j + 2 * h) = o;
  }
  if (g == 0 && lane == 0) __builtin_amdgcn_raw_buffer_store_b32(tag, rws, WS_E, 0, AUX_SC1);
  STAMP(11);
  STFLUSH();
}

extern "C" void kernel_launch(void* const* d_in, const int* in_sizes, int n_in,
                              void* d_out, int out_size, void* d_ws, size_t ws_size,
                              hipStream_t stream) {
  constexpr int LDS_BYTES = 84 * 1024;
  (void)hipFuncSetAttribute((const void*)fused_decode, hipFuncAttributeMaxDynamicSharedMemorySize, LDS_BYTES);
  fused_decode<<<256, NTHR, LDS_BYTES, stream>>>((const u16*)d_in[0], (const u16*)d_in[1], (const u16*)d_in[2], (const u16*)d_in[3],
      (const u16*)d_in[4], (const u16*)d_in[6], (const u16*)d_in[7], (const u16*)d_in[8], (u16*)d_out, (unsigned char*)d_ws);
}
```

```cpp
#include <hip/hip_runtime.h>
#include <hip/hip_bf16.h>
#include <stdint.h>

typedef unsigned short u16;
typedef unsigned u32;
typedef u32 u32x4 __attribute__((ext_vector_type(4)));
typedef u32 u32x2 __attribute__((ext_vector_type(2)));
typedef float f32x4 __attribute__((ext_vector_type(4)));
typedef float f32x2 __attribute__((ext_vector_type(2)));
typedef __bf16 bf16x2 __attribute__((ext_vector_type(2)));
typedef __bf16 bf16x8 __attribute__((ext_vector_type(8)));
#define GAS __attribute__((address_space(1)))
#define LAS __attribute__((address_space(3)))

constexpr int D = 512, NH = 32, DH = 128, SK = 4096, WIN = 1024, FF = 1376;
constexpr int CODE_BYTES = 12 * 1024;
constexpr int NTHR = 640;
#define KWIN 5
#define VWIN 6
#define KSIG 3
#define KQT 15
#define X2D 8
#define X3D 12
#define FWIN 4
constexpr int AUX_NT = 2, AUX_SC1 = 16;
constexpr u32 SPIN_CAP = 1u << 13;
constexpr u32 LSPIN_CAP = 1u << 17;

constexpr u32 WS_E = 0;
constexpr u32 WS_X1 = 4096;
constexpr u32 WS_X2 = WS_X1 + 32 * 8 * 2 * 1024;
constexpr u32 WS_X3 = WS_X2 + 32 * 8 * 2048;
constexpr u32 WS_END = WS_X3 + 8 * 32 * 1024;

constexpr int L_QF = 0;
constexpr int L_FF = 1;
constexpr int L_KQ = 2;
constexpr int L_PC = 3;
constexpr int L_X2 = 4;
constexpr int L_TAG = 5;
constexpr int L_MX = 8;
constexpr int L_KI = 16;
constexpr int L_PR = 20;
constexpr int L_VI = 24;
constexpr int L_QPK = 32;
constexpr int L_HPK = 96;
constexpr int L_OPK = 160;
constexpr int L_YF = 288;
constexpr int L_PL = 416;
constexpr int L_P = 432;
constexpr int L_PART = 688;
constexpr int L_DW = L_PART + 512;

__device__ __forceinline__ float bflo(u32 v) { return __uint_as_float(v << 16); }
__device__ __forceinline__ float bfhi(u32 v) { return __uint_as_float(v & 0xffff0000u); }
__device__ __forceinline__ u32 pk2(float lo, float hi) {
  bf16x2 r = {(__bf16)lo, (__bf16)hi};
  return __builtin_bit_cast(u32, r);
}
__device__ __forceinline__ float rbf(float f) { return bflo(pk2(f, f)); }
template <int CTRL> __device__ __forceinline__ float dpp(float x) {
  return __builtin_bit_cast(float, __builtin_amdgcn_mov_dpp(__builtin_bit_cast(int, x), CTRL, 0xf, 0xf, true));
}
constexpr int XOR1 = 0xB1, XOR2 = 0x4E, XOR7 = 0x141, ROR8 = 0x128, ROR4 = 0x124;
__device__ __forceinline__ float xrow_sum(float x) {
  auto s = __builtin_amdgcn_permlane16_swap(__float_as_uint(x), __float_as_uint(x), false, false);
  x = __uint_as_float(s[0]) + __uint_as_float(s[1]);
  auto t = __builtin_amdgcn_permlane32_swap(__float_as_uint(x), __float_as_uint(x), false, false);
  return __uint_as_float(t[0]) + __uint_as_float(t[1]);
}
__device__ __forceinline__ float xrow_max(float x) {
  auto s = __builtin_amdgcn_permlane16_swap(__float_as_uint(x), __float_as_uint(x), false, false);
  x = fmaxf(__uint_as_float(s[0]), __uint_as_float(s[1]));
  auto t = __builtin_amdgcn_permlane32_swap(__float_as_uint(x), __float_as_uint(x), false, false);
  return fmaxf(__uint_as_float(t[0]), __uint_as_float(t[1]));
}
__device__ __forceinline__ float wave_sum(float x) {
  x += dpp<XOR1>(x); x += dpp<XOR2>(x); x += dpp<XOR7>(x); x += dpp<ROR8>(x);
  return xrow_sum(x);
}
__device__ __forceinline__ float wave_max(float x) {
  x = fmaxf(x, dpp<XOR1>(x)); x = fmaxf(x, dpp<XOR2>(x)); x = fmaxf(x, dpp<XOR7>(x)); x = fmaxf(x, dpp<ROR8>(x));
  return xrow_max(x);
}
__device__ __forceinline__ float dot2(u32 a, u32 b, float c) {
  return __builtin_amdgcn_fdot2_f32_bf16(__builtin_bit_cast(bf16x2, a), __builtin_bit_cast(bf16x2, b), c, false);
}
typedef __amdgpu_buffer_rsrc_t rsrc_t;
__device__ __forceinline__ rsrc_t mk_rsrc(const void* p, u32 bytes) {
  return __builtin_amdgcn_make_buffer_rsrc((void*)p, 0, bytes, 0x00020000);
}
__device__ __forceinline__ u32x4 ld_nt(rsrc_t r, u32 voff, u32 soff) { return __builtin_amdgcn_raw_buffer_load_b128(r, voff, soff, AUX_NT); }
__device__ __forceinline__ u32x4 ld_pl(rsrc_t r, u32 voff, u32 soff) { return __builtin_amdgcn_raw_buffer_load_b128(r, voff, soff, 0); }
__device__ __forceinline__ u32x4 ld_sc(rsrc_t r, u32 voff, u32 soff) { return __builtin_amdgcn_raw_buffer_load_b128(r, voff, soff, AUX_SC1); }
__device__ __forceinline__ void st_unit(rsrc_t r, u32 voff, float a, float b, u32 tag) {
  u32x4 v = {__float_as_uint(a), tag, __float_as_uint(b), tag};
  __builtin_amdgcn_raw_buffer_store_b128(v, r, voff, 0, AUX_SC1);
}
__device__ __forceinline__ f32x4 mfma(u32x4 a, u32x4 b, f32x4 c) {
  return __builtin_amdgcn_mfma_f32_16x16x32_bf16(__builtin_bit_cast(bf16x8, a), __builtin_bit_cast(bf16x8, b), c, 0, 0, 0);
}
template <int N> __device__ __forceinline__ void waitv() { asm volatile("s_waitcnt vmcnt(%0)" :: "i"(N) : "memory"); }
#define CBAR() asm volatile("" ::: "memory")
#define SCHEDB() __builtin_amdgcn_sched_barrier(0)
__device__ __forceinline__ u32 lds_ld(volatile LAS u32* p) { return *p; }

#define STAMP(i) do {} while (0)
#define STFLUSH() do {} while (0)
#define STDECL() do {} while (0)

__device__ __forceinline__ void tr8(const u32x4* W, u32x4* Bf) {
#pragma unroll
  for (int q = 0; q < 4; ++q) {
#pragma unroll
    for (int p = 0; p < 4; ++p) {
      Bf[2 * q][p] = __builtin_amdgcn_perm(W[2 * p + 1][q], W[2 * p][q], 0x05040100u);
      Bf[2 * q + 1][p] = __builtin_amdgcn_perm(W[2 * p + 1][q], W[2 * p][q], 0x07060302u);
    }
  }
}
__device__ __forceinline__ void epi(const f32x4* acc, int lane, float& e0, float& e1) {
  const bool lo = (lane & 8) == 0, g1 = (lane & 16) != 0, g2 = (lane & 32) != 0;
  float v[8];
#pragma unroll
  for (int j = 0; j < 8; ++j) {
    const float r0 = acc[j][0], r1 = acc[j][1], r2 = acc[j][2], r3 = acc[j][3];
    const float a = r0 + dpp<ROR8>(r1), b = r3 + dpp<ROR8>(r2);
    v[j] = lo ? a : b;
  }
  const float p0 = g1 ? v[2] : v[0], p1 = g1 ? v[6] : v[4], q0 = g1 ? v[3] : v[1], q1 = g1 ? v[7] : v[5];
  e0 = g2 ? p1 : p0;
  e1 = g2 ? q1 : q0;
}

__global__ void __launch_bounds__(NTHR, 3)
fused_decode(const u16* __restrict__ x, const u16* __restrict__ kc, const u16* __restrict__ vc,
             const u16* __restrict__ wq, const u16* __restrict__ wo, const u16* __restrict__ wg,
             const u16* __restrict__ wu, const u16* __restrict__ wd, u16* __restrict__ out, unsigned char* __restrict__ ws) {
  extern __shared__ __attribute__((aligned(16))) u32 lds_raw[];
  LAS u32* lds = (LAS u32*)lds_raw;
  const int tid = threadIdx.x, lane = tid & 63;
  const int wave = __builtin_amdgcn_readfirstlane(tid >> 6);
  STDECL();
  lds[tid & 31] = 0u;
  {
    const u32 voff = ((blockIdx.x >> 3) * 4 + wave) * 1024 + lane * 16;
    asm volatile("s_cmp_lt_u32 %1, 32\n\ts_cbranch_scc0 .Lcpf_%=\n\ts_cmp_lt_u32 %2, 4\n\ts_cbranch_scc0 .Lcpf_%=\n\t"
                 "s_getpc_b64 s[60:61]\n\ts_and_b32 s61, s61, 0xffff\n\ts_mov_b32 s62, %3\n\ts_mov_b32 s63, 0x20000\n\t"
                 "s_mov_b32 s59, m0\n\ts_lshl_b32 s58, %2, 10\n\ts_add_u32 s58, s58, 0x4000\n\ts_mov_b32 m0, s58\n\ts_nop 0\n\t"
                 "buffer_load_dwordx4 %0, s[60:63], 0 offen lds\n\ts_mov_b32 m0, s59\n.Lcpf_%=:"
                 :: "v"(voff), "s"(blockIdx.x), "s"(wave), "i"(CODE_BYTES) : "s58", "s59", "s60", "s61", "s62", "s63", "scc", "memory");
    SCHEDB();
  }
  asm volatile("s_waitcnt lgkmcnt(0)" :: "s"(x), "s"(kc), "s"(vc), "s"(wq), "s"(wo), "s"(wg), "s"(wu), "s"(wd), "s"(out), "s"(ws) : "memory");
  STAMP(0);
  const int g = blockIdx.x;
  const int h = (g & 7) * 4 + (g >> 6), j = (g >> 3) & 7;

  if (wave >= 2) {
    const int sw = (wave - 2) & 3;
    const int b = j >> 2, s = j & 3;
    const size_t kvoff = ((size_t)(b * NH + h) * SK + (SK - WIN) + 256 * s + 64 * sw) * DH;
    const u32 vo = lane * 16;
    const int c = lane & 15;
#define KVOFF(t) ((t) * 1024)
    if (wave < 6) {
      const rsrc_t rk = mk_rsrc(kc + kvoff, 16384);
      u32x4 K[16];
      asm volatile("s_barrier" ::: "memory");
      SCHEDB();
#pragma unroll
      for (int t = 0; t < 16; ++t) {
        if (t >= KWIN) waitv<KWIN - 1>();
        if (t == KWIN) STAMP(5);
        if (t == 10) STAMP(6);
        SCHEDB();
        K[t] = ld_nt(rk, vo, KVOFF(t));
        SCHEDB();
        if (t == KQT) { if (sw == 0 && lane == 0) *(volatile LAS u32*)(lds + L_KQ) = 1u; SCHEDB(); }
      }
      waitv<KSIG>();
      if (lane == 0) *(volatile LAS u32*)(lds + L_KI + sw) = 1u;
      STAMP(1);
      for (u32 sp = 0; lds_ld(lds + L_QF) == 0u && sp < LSPIN_CAP; ++sp) __builtin_amdgcn_s_sleep(1);
      CBAR();
      STAMP(2);
      const u32x4 q = *(LAS u32x4*)(lds + L_QPK + 4 * c);
      float xs[16];
#pragma unroll
      for (int t = 0; t < 16; ++t) {
        float d = dot2(K[t][0], q[0], 0.f);
        d = dot2(K[t][1], q[1], d); d = dot2(K[t][2], q[2], d); xs[t] = dot2(K[t][3], q[3], d);
      }
#define TR_STEP(HALF, CTRL, BIT) _Pragma("unroll") for (int u = 0; u < (HALF); ++u) { \
        const bool up_ = (c & (BIT)) != 0; const float keep_ = up_ ? xs[u + (HALF)] : xs[u], send_ = up_ ? xs[u] : xs[u + (HALF)]; \
        xs[u] = keep_ + dpp<CTRL>(send_); }
      TR_STEP(8, ROR8, 8) TR_STEP(4, XOR7, 4) TR_STEP(2, XOR2, 2) TR_STEP(1, XOR1, 1)
#undef TR_STEP
      const float sc = rbf(rbf(xs[0]) * 0.08838834764831845f);
      const float mw = wave_max(sc);
      if (lane == 0) { *(LAS u32x2*)(lds + L_MX + 2 * sw) = u32x2{__float_as_uint(mw), 1u}; }
      STAMP(3);
      float M;
      for (u32 sp = 0;; ++sp) {
        CBAR();
        const u32x4 a = *(volatile LAS u32x4*)(lds + L_MX), bq = *(volatile LAS u32x4*)(lds + L_MX + 4);
        M = fmaxf(fmaxf(__uint_as_float(a[0]), __uint_as_float(a[2])), fmaxf(__uint_as_float(bq[0]), __uint_as_float(bq[2])));
        if ((a[1] & a[3] & bq[1] & bq[3]) != 0u || sp >= LSPIN_CAP) break;
      }
      const float p = __builtin_amdgcn_exp2f((sc - M) * 1.4426950408889634f);
      lds[L_P + 64 * sw + lane] = __float_as_uint(p);
      const float lw = wave_sum(p);
      if (lane == 0) lds[L_PL + sw] = __float_as_uint(lw);
      CBAR();
      if (lane == 0) *(volatile LAS u32*)(lds + L_PR + sw) = 1u;
      STAMP(4);
      STFLUSH();
      return;
    }
    const rsrc_t rv = mk_rsrc(vc + kvoff, 16384);
    u32x4 V[16];
    asm volatile("s_barrier" ::: "memory");
    for (u32 sp = 0; lds_ld(lds + L_KI + sw) == 0u && sp < LSPIN_CAP; ++sp) __builtin_amdgcn_s_sleep(2);
    SCHEDB();
    STAMP(1);
#pragma unroll
    for (int t = 0; t < 16; ++t) {
      if (t >= VWIN) waitv<VWIN - 1>();
      SCHEDB();
      V[t] = ld_nt(rv, vo, KVOFF(t));
      SCHEDB();
    }
    if (lane == 0) *(volatile LAS u32*)(lds + L_VI + sw) = 1u;
    STAMP(2);
    for (u32 sp = 0; lds_ld(lds + L_PR + sw) == 0u && sp < LSPIN_CAP; ++sp) __builtin_amdgcn_s_sleep(1);
    CBAR();
    const float p = __uint_as_float(lds[L_P + 64 * sw + lane]);
    STAMP(3);
    float acc[8];
#pragma unroll
    for (int k = 0; k < 8; ++k) acc[k] = 0.f;
#define PV_STEP(T) { const float pb = dpp<0x150 + (T)>(p); \
      _Pragma("unroll") for (int k = 0; k < 4; ++k) { acc[2 * k] = fmaf(pb, bflo(V[T][k]), acc[2 * k]); acc[2 * k + 1] = fmaf(pb, bfhi(V[T][k]), acc[2 * k + 1]); } }
    PV_STEP(0) PV_STEP(1) PV_STEP(2) PV_STEP(3) PV_STEP(4) PV_STEP(5) PV_STEP(6) PV_STEP(7)
    PV_STEP(8) PV_STEP(9) PV_STEP(10) PV_STEP(11) PV_STEP(12) PV_STEP(13) PV_STEP(14) PV_STEP(15)
#undef PV_STEP
    float v1[4];
#pragma unroll
    for (int k = 0; k < 4; ++k) {
      auto sw2 = __builtin_amdgcn_permlane32_swap(__float_as_uint(acc[k]), __float_as_uint(acc[k + 4]), false, false);
      v1[k] = __uint_as_float(sw2[0]) + __uint_as_float(sw2[1]);
    }
    auto s0 = __builtin_amdgcn_permlane16_swap(__float_as_uint(v1[0]), __float_as_uint(v1[2]), false, false);
    auto s1 = __builtin_amdgcn_permlane16_swap(__float_as_uint(v1[1]), __float_as_uint(v1[3]), false, false);
    const float o0 = __uint_as_float(s0[0]) + __uint_as_float(s0[1]), o1 = __uint_as_float(s1[0]) + __uint_as_float(s1[1]);
    *(LAS f32x2*)(lds + L_PART + 128 * sw + 8 * c + 2 * (lane >> 4)) = f32x2{o0, o1};
    CBAR();
    u32 arr = 0;
    if (lane == 0) arr = __hip_atomic_fetch_add(lds + L_PC, 1u, __ATOMIC_RELAXED, __HIP_MEMORY_SCOPE_WORKGROUP);
    arr = __builtin_amdgcn_readfirstlane(arr);
    if (arr == 3u) {
      CBAR();
      const f32x2 p0 = *(LAS f32x2*)(lds + L_PART + 2 * lane), p1 = *(LAS f32x2*)(lds + L_PART + 128 + 2 * lane),
                  p2 = *(LAS f32x2*)(lds + L_PART + 256 + 2 * lane), p3 = *(LAS f32x2*)(lds + L_PART + 384 + 2 * lane);
      const u32x4 a = *(LAS u32x4*)(lds + L_MX), bq = *(LAS u32x4*)(lds + L_MX + 4), lq = *(LAS u32x4*)(lds + L_PL);
      const u32 tag = lds[L_TAG];
      const rsrc_t rws = mk_rsrc(ws, WS_END);
      const u32 xb = WS_X2 + (h * 8 + j) * 2048;
      st_unit(rws, xb + lane * 16, (p0[0] + p1[0]) + (p2[0] + p3[0]), (p0[1] + p1[1]) + (p2[1] + p3[1]), tag);
      const float Mcu = fmaxf(fmaxf(__uint_as_float(a[0]), __uint_as_float(a[2])), fmaxf(__uint_as_float(bq[0]), __uint_as_float(bq[2])));
      const float Lcu = (__uint_as_float(lq[0]) + __uint_as_float(lq[1])) + (__uint_as_float(lq[2]) + __uint_as_float(lq[3]));
      if (lane == 0) { st_unit(rws, xb + 1024, Mcu, Lcu, tag); *(volatile LAS u32*)(lds + L_X2) = 1u; }
    }
    STAMP(4);
    STFLUSH();
    return;
  }

  __builtin_amdgcn_s_setprio(3);
  const int f = wave;
  const rsrc_t rws = mk_rsrc(ws, WS_END);
  const int m4 = lane & 3, gq = lane >> 4, rho = lane >> 3, chunk = lane & 7;
  const int ab = (m4 >> 1), ahi = m4 & 1;
  const int kq = j >> 1;
  const int nch = h < 12 ? 6 : 5, c0 = h < 12 ? 6 * h : 72 + 5 * (h - 12);
  const rsrc_t rx = mk_rsrc(x, 2048);
  const u32x4 xs0 = ld_pl(rx, lane * 16, 0), xs1 = ld_pl(rx, lane * 16, 1024);
  const u32 xaoff = ab * 1024 + (128 * kq + 16 * gq + 8 * ahi) * 2;
  const u32x4 xa0 = ld_pl(rx, xaoff, 0), xa1 = ld_pl(rx, xaoff, 128);
  const u32 E = __builtin_amdgcn_raw_buffer_load_b32(rws, WS_E, 0, AUX_SC1);
  const u32 xfin = __builtin_amdgcn_raw_buffer_load_b32(rx, (lane & 1) * 1024 + (64 * j + 2 * h) * 2, 0, 0);
  u32x4 W[16];
  {
    const u16* pa = f == 0 ? wq + (size_t)(128 * kq) * 4096 + h * 128 + 64 * (j & 1)
                           : ((j & 1) ? wu : wg) + (size_t)(128 * kq) * FF + 8 * c0;
    const u32 strb = f == 0 ? 8192u : 2752u;
    const int ch = f == 0 ? chunk : (chunk < nch ? chunk : nch - 1);
    const rsrc_t ra = mk_rsrc(pa, 128u * strb);
    const u32 vo = rho * 8 * strb + ch * 16;
#pragma unroll
    for (int t = 0; t < 16; ++t) {
      W[t] = ld_nt(ra, vo, ((t >> 3) * 64 + (t & 7)) * strb);
      SCHEDB(); }
  }
  asm volatile("s_barrier" ::: "memory");
  SCHEDB();
  STAMP(1);
  float mb, rd;
  {
    float s0 = 0.f, s1 = 0.f;
#pragma unroll
    for (int k = 0; k < 4; ++k) { s0 += bflo(xs0[k]) + bfhi(xs0[k]); s1 += bflo(xs1[k]) + bfhi(xs1[k]); }
    const float mean0 = wave_sum(s0) * (1.f / 512.f), mean1 = wave_sum(s1) * (1.f / 512.f);
    float q0 = 0.f, q1 = 0.f;
#pragma unroll
    for (int k = 0; k < 4; ++k) {
      float a = bflo(xs0[k]) - mean0, bb = bfhi(xs0[k]) - mean0; q0 = fmaf(a, a, q0); q0 = fmaf(bb, bb, q0);
      a = bflo(xs1[k]) - mean1; bb = bfhi(xs1[k]) - mean1; q1 = fmaf(a, a, q1); q1 = fmaf(bb, bb, q1);
    }
    const float var0 = wave_sum(q0) * (1.f / 512.f), var1 = wave_sum(q1) * (1.f / 512.f);
    const float mean = ab ? mean1 : mean0, var = ab ? var1 : var0;
    mb = rbf(mean);
    rd = rbf(__builtin_amdgcn_sqrtf(rbf(rbf(var) + 1e-5f)));
  }
  const float rinv = __builtin_amdgcn_rcpf(rd);
  u32x4 A0, A1;
#pragma unroll
  for (int k = 0; k < 4; ++k) {
    float v[4] = {rbf(bflo(xa0[k]) - mb), rbf(bfhi(xa0[k]) - mb), rbf(bflo(xa1[k]) - mb), rbf(bfhi(xa1[k]) - mb)};
#pragma unroll
    for (int e = 0; e < 4; ++e) { const float q0 = v[e] * rinv; const float r = fmaf(-q0, rd, v[e]); v[e] = fmaf(r, rinv, q0); }
    A0[k] = pk2(v[0], v[1]); A1[k] = pk2(v[2], v[3]);
  }
  STAMP(2);
  const u32 tag = E + 1u;
  float e0, e1;
  {
    f32x4 acc[8];
    u32x4 Bf[8];
    tr8(W, Bf);
#pragma unroll
    for (int t = 0; t < 8; ++t) acc[t] = mfma(A0, Bf[t], f32x4{0.f, 0.f, 0.f, 0.f});
    tr8(W + 8, Bf);
#pragma unroll
    for (int t = 0; t < 8; ++t) acc[t] = mfma(A1, Bf[t], acc[t]);
    epi(acc, lane, e0, e1);
  }
  STAMP(13);
  const u32 uidx = ((lane >> 3) & 1) * 32 + 4 * (lane & 7) + (lane >> 4);
  st_unit(rws, WS_X1 + ((h * 8 + j) * 2 + f) * 1024 + uidx * 16, e0, e1, tag);
  STAMP(3);
  if (f == 1) {
    float a0x, a0y, a1x, a1y;
    for (u32 sp = 0; lds_ld(lds + L_KI) == 0u && sp < LSPIN_CAP; ++sp) __builtin_amdgcn_s_sleep(2);
    for (u32 sp = 0;; ++sp) {
      CBAR();
      u32x4 u[8];
#pragma unroll
      for (int k = 0; k < 8; ++k) u[k] = ld_sc(rws, WS_X1 + (h * 16 + 1) * 1024 + lane * 16, k * 2048);
      bool ok = true;
#pragma unroll
      for (int k = 0; k < 8; ++k) ok = ok && u[k][1] == tag && u[k][3] == tag;
      a0x = (__uint_as_float(u[0][0]) + __uint_as_float(u[2][0])) + (__uint_as_float(u[4][0]) + __uint_as_float(u[6][0]));
      a0y = (__uint_as_float(u[0][2]) + __uint_as_float(u[2][2])) + (__uint_as_float(u[4][2]) + __uint_as_float(u[6][2]));
      a1x = (__uint_as_float(u[1][0]) + __uint_as_float(u[3][0])) + (__uint_as_float(u[5][0]) + __uint_as_float(u[7][0]));
      a1y = (__uint_as_float(u[1][2]) + __uint_as_float(u[3][2])) + (__uint_as_float(u[5][2]) + __uint_as_float(u[7][2]));
      if (__all(ok) || sp >= SPIN_CAP) break;
    }
    STAMP(4);
    const float g0 = rbf(a0x), g1 = rbf(a0y), u0 = rbf(a1x), u1 = rbf(a1y);
    const float s0 = rbf(__builtin_amdgcn_rcpf(1.f + __builtin_amdgcn_exp2f(-g0 * 1.4426950408889634f)));
    const float s1 = rbf(__builtin_amdgcn_rcpf(1.f + __builtin_amdgcn_exp2f(-g1 * 1.4426950408889634f)));
    const u32 hp = (lane & 31) < 4 * nch ? pk2(s0 * u0, s1 * u1) : 0u;
    lds[L_HPK + lane] = hp;
    for (u32 sp = 0;; ++sp) {
      CBAR();
      const u32x4 a = *(volatile LAS u32x4*)(lds + L_VI);
      if ((a[0] & a[1] & a[2] & a[3]) != 0u || sp >= LSPIN_CAP) break;
      __builtin_amdgcn_s_sleep(2);
    }
    {
      const u16* pb = wd + (size_t)(8 * c0) * D + 64 * j;
      const int rr = rho < nch ? rho : nch - 1;
      const rsrc_t rb = mk_rsrc(pb, 64u * 1024u);
      const u32 vo = rr * 8192 + chunk * 16;
#pragma unroll
      for (int t = 0; t < 8; ++t) { W[t] = ld_nt(rb, vo, t * 1024); SCHEDB(); }
    }
    CBAR();
    const u32x4 Ah = *(LAS u32x4*)(lds + L_HPK + ab * 32 + 8 * gq + 4 * ahi);
    f32x4 acc[8];
    u32x4 Bf[8];
    tr8(W, Bf);
#pragma unroll
    for (int t = 0; t < 8; ++t) acc[t] = mfma(Ah, Bf[t], f32x4{0.f, 0.f, 0.f, 0.f});
    epi(acc, lane, e0, e1);
    *(LAS f32x2*)(lds + L_YF + 2 * lane) = f32x2{e0, e1};
    CBAR();
    if (lane == 0) *(volatile LAS u32*)(lds + L_FF) = 1u;
    STAMP(6);
    STFLUSH();
    return;
  }
  {
    float qa, qb;
    for (u32 sp = 0; lds_ld(lds + L_KQ) == 0u && sp < LSPIN_CAP; ++sp) __builtin_amdgcn_s_sleep(2);
    STAMP(14);
    for (u32 sp = 0;; ++sp) {
      CBAR();
      u32x4 u[4];
#pragma unroll
      for (int k = 0; k < 4; ++k) u[k] = ld_sc(rws, WS_X1 + (h * 16 + 2 * (lane >> 5)) * 1024 + ((j >> 2) * 32 + (lane & 31)) * 16, k * 4096);
      bool ok = true;
#pragma unroll
      for (int k = 0; k < 4; ++k) ok = ok && u[k][1] == tag && u[k][3] == tag;
      qa = (__uint_as_float(u[0][0]) + __uint_as_float(u[1][0])) + (__uint_as_float(u[2][0]) + __uint_as_float(u[3][0]));
      qb = (__uint_as_float(u[0][2]) + __uint_as_float(u[1][2])) + (__uint_as_float(u[2][2]) + __uint_as_float(u[3][2]));
      if (__all(ok) || sp >= SPIN_CAP) break;
    }
    lds[L_QPK + lane] = pk2(qa, qb);
    if (lane == 0) lds[L_TAG] = tag;
    CBAR();
    if (lane == 0) *(volatile LAS u32*)(lds + L_QF) = 1u;
  }
  STAMP(4);
  for (u32 sp = 0;; ++sp) {
    CBAR();
    const u32x4 a = *(volatile LAS u32x4*)(lds + L_VI);
    if ((a[0] & a[1] & a[2] & a[3]) != 0u || sp >= LSPIN_CAP) break;
    __builtin_amdgcn_s_sleep(2);
  }
  {
    const rsrc_t rb = mk_rsrc(wo + (size_t)(h * 128) * D + 64 * j, 128u * 1024u);
    const u32 vo = rho * 8192 + chunk * 16;
#pragma unroll
    for (int t = 0; t < 16; ++t) { W[t] = ld_nt(rb, vo, (t >> 3) * 65536 + (t & 7) * 1024); SCHEDB(); }
  }
  STAMP(5);
  for (u32 sp = 0; lds_ld(lds + L_X2) == 0u && sp < LSPIN_CAP; ++sp) {}
  STAMP(7);
  __builtin_amdgcn_s_sleep(X2D);
  u32x4 u[8], us;
#define X2_ISSUE() do { us = ld_sc(rws, WS_X2 + (h * 8 + (lane & 7)) * 2048 + 1024, 0); \
    _Pragma("unroll") for (int k = 0; k < 8; ++k) u[k] = ld_sc(rws, WS_X2 + h * 8 * 2048 + lane * 16, k * 2048); SCHEDB(); } while (0)
  X2_ISSUE();
  u32x4 B0[8], B1[8];
  tr8(W, B0); tr8(W + 8, B1);
#pragma unroll
  for (int t = 0; t < 8; ++t) asm volatile("" : "+v"(B0[t]), "+v"(B1[t]));
  STAMP(6);
  for (u32 sp = 0; lds_ld(lds + L_FF) == 0u && sp < LSPIN_CAP; ++sp) {}
  CBAR();
  f32x2 yf = *(LAS f32x2*)(lds + L_YF + 2 * lane);
  u32 x3off = WS_X3 + ((j * 32 + 4 * (lane & 7) + (lane >> 4)) * 64 + 2 * h + ((lane >> 3) & 1)) * 16;
  asm volatile("" : "+v"(yf), "+v"(x3off));
  float coef;
  for (u32 sp = 0;; ++sp) {
    const float m = __uint_as_float(us[0]), l = __uint_as_float(us[2]);
    float Mb = fmaxf(m, dpp<XOR1>(m)); Mb = fmaxf(Mb, dpp<XOR2>(Mb));
    const float w = __builtin_amdgcn_exp2f((m - Mb) * 1.4426950408889634f);
    float L = w * l; L += dpp<XOR1>(L); L += dpp<XOR2>(L);
    coef = w * __builtin_amdgcn_rcpf(L);
    asm volatile("" : "+v"(coef));
    SCHEDB();
    bool ok = us[1] == tag && us[3] == tag;
#pragma unroll
    for (int k = 0; k < 8; ++k) ok = ok && u[k][1] == tag && u[k][3] == tag;
    if (__all(ok) || sp >= SPIN_CAP) {
      break; }
    CBAR();
    X2_ISSUE();
  }
#undef X2_ISSUE
  STAMP(8);
  {
    float o00 = 0.f, o01 = 0.f, o10 = 0.f, o11 = 0.f;
#pragma unroll
    for (int k = 0; k < 4; ++k) {
      const float ca = __uint_as_float(__builtin_amdgcn_readlane(__float_as_uint(coef), k));
      const float cb = __uint_as_float(__builtin_amdgcn_readlane(__float_as_uint(coef), 4 + k));
      o00 = fmaf(ca, __uint_as_float(u[k][0]), o00); o01 = fmaf(ca, __uint_as_float(u[k][2]), o01);
      o10 = fmaf(cb, __uint_as_float(u[4 + k][0]), o10); o11 = fmaf(cb, __uint_as_float(u[4 + k][2]), o11);
    }
    lds[L_OPK + lane] = pk2(o00, o01);
    lds[L_OPK + 64 + lane] = pk2(o10, o11);
  }
  CBAR();
  {
    const u32x4 Ao0 = *(LAS u32x4*)(lds + L_OPK + ab * 64 + 8 * gq + 4 * ahi);
    const u32x4 Ao1 = *(LAS u32x4*)(lds + L_OPK + ab * 64 + 32 + 8 * gq + 4 * ahi);
    f32x4 acc[8];
#pragma unroll
    for (int t = 0; t < 8; ++t) acc[t] = mfma(Ao0, B0[t], f32x4{0.f, 0.f, 0.f, 0.f});
#pragma unroll
    for (int t = 0; t < 8; ++t) acc[t] = mfma(Ao1, B1[t], acc[t]);
    epi(acc, lane, e0, e1);
  }
  st_unit(rws, x3off, e0 + yf[0], e1 + yf[1], tag);
  STAMP(9);
  __builtin_amdgcn_s_sleep(X3D);
  float y0, y1;
  for (u32 sp = 0;; ++sp) {
    CBAR();
    const u32x4 v = ld_sc(rws, WS_X3 + (j * 32 + h) * 1024 + lane * 16, 0);
    y0 = __uint_as_float(v[0]); y1 = __uint_as_float(v[2]);
    if (__all(v[1] == tag && v[3] == tag) || sp >= SPIN_CAP) {
      break; }
  }
  STAMP(10);
  y0 += dpp<XOR2>(y0); y1 += dpp<XOR2>(y1);
  y0 += dpp<ROR4>(y0); y1 += dpp<ROR4>(y1);
  y0 += dpp<ROR8>(y0); y1 += dpp<ROR8>(y1);
  y0 = xrow_sum(y0); y1 = xrow_sum(y1);
  if (lane < 2) {
    const u32 o = pk2(bflo(xfin) + y0, bfhi(xfin) + y1);
    *(GAS u32*)(out + lane * D + 64 * j + 2 * h) = o;
  }
  if (g == 0 && lane == 0) __builtin_amdgcn_raw_buffer_store_b32(tag, rws, WS_E, 0, AUX_SC1);
  STAMP(11);
  STFLUSH();
}

extern "C" void kernel_launch(void* const* d_in, const int* in_sizes, int n_in,
                              void* d_out, int out_size, void* d_ws, size_t ws_size,
                              hipStream_t stream) {
  constexpr int LDS_BYTES = 84 * 1024;
  (void)hipFuncSetAttribute((const void*)fused_decode, hipFuncAttributeMaxDynamicSharedMemorySize, LDS_BYTES);
  fused_decode<<<256, NTHR, LDS_BYTES, stream>>>((const u16*)d_in[0], (const u16*)d_in[1], (const u16*)d_in[2], (const u16*)d_in[3],
      (const u16*)d_in[4], (const u16*)d_in[6], (const u16*)d_in[7], (const u16*)d_in[8], (u16*)d_out, (unsigned char*)d_ws);
}
```

```cpp
#include <hip/hip_runtime.h>
#include <hip/hip_bf16.h>
#include <stdint.h>

typedef unsigned short u16;
typedef unsigned u32;
typedef u32 u32x4 __attribute__((ext_vector_type(4)));
typedef u32 u32x2 __attribute__((ext_vector_type(2)));
typedef float f32x4 __attribute__((ext_vector_type(4)));
typedef float f32x2 __attribute__((ext_vector_type(2)));
typedef __bf16 bf16x2 __attribute__((ext_vector_type(2)));
typedef __bf16 bf16x8 __attribute__((ext_vector_type(8)));
#define GAS __attribute__((address_space(1)))
#define LAS __attribute__((address_space(3)))

constexpr int D = 512, NH = 32, DH = 128, SK = 4096, WIN = 1024, FF = 1376;
constexpr int CODE_BYTES = 12 * 1024;
constexpr int NTHR = 640;
constexpr int KWIN = 5, VWIN = 6;
constexpr int KSIG = 3;
constexpr int KQT = 15;
constexpr int X2D = 8, X3D = 12;
constexpr int AUX_NT = 2, AUX_SC1 = 16;
constexpr u32 SPIN_CAP = 1u << 16;
constexpr u32 LSPIN_CAP = 1u << 21;

constexpr u32 WS_E = 0;
constexpr u32 WS_X1 = 4096;
constexpr u32 WS_X2 = WS_X1 + 32 * 8 * 2 * 1024;
constexpr u32 WS_X3 = WS_X2 + 32 * 8 * 2048;
constexpr u32 WS_END = WS_X3 + 8 * 32 * 1024;

constexpr int L_QF = 0;
constexpr int L_FF = 1;
constexpr int L_KQ = 2;
constexpr int L_PC = 3;
constexpr int L_X2 = 4;
constexpr int L_TAG = 5;
constexpr int L_MX = 8;
constexpr int L_KI = 16;
constexpr int L_PR = 20;
constexpr int L_VI = 24;
constexpr int L_QPK = 32;
constexpr int L_HPK = 96;
constexpr int L_OPK = 160;
constexpr int L_YF = 288;
constexpr int L_PL = 416;
constexpr int L_P = 432;
constexpr int L_PART = 688;
constexpr int L_DW = L_PART + 512;

__device__ __forceinline__ float bflo(u32 v) { return __uint_as_float(v << 16); }
__device__ __forceinline__ float bfhi(u32 v) { return __uint_as_float(v & 0xffff0000u); }
__device__ __forceinline__ u32 pk2(float lo, float hi) {
  bf16x2 r = {(__bf16)lo, (__bf16)hi};
  return __builtin_bit_cast(u32, r);
}
__device__ __forceinline__ float rbf(float f) { return bflo(pk2(f, f)); }
template <int CTRL> __device__ __forceinline__ float dpp(float x) {
  return __builtin_bit_cast(float, __builtin_amdgcn_mov_dpp(__builtin_bit_cast(int, x), CTRL, 0xf, 0xf, true));
}
constexpr int XOR1 = 0xB1, XOR2 = 0x4E, XOR7 = 0x141, ROR8 = 0x128, ROR4 = 0x124;
__device__ __forceinline__ float xrow_sum(float x) {
  auto s = __builtin_amdgcn_permlane16_swap(__float_as_uint(x), __float_as_uint(x), false, false);
  x = __uint_as_float(s[0]) + __uint_as_float(s[1]);
  auto t = __builtin_amdgcn_permlane32_swap(__float_as_uint(x), __float_as_uint(x), false, false);
  return __uint_as_float(t[0]) + __uint_as_float(t[1]);
}
__device__ __forceinline__ float xrow_max(float x) {
  auto s = __builtin_amdgcn_permlane16_swap(__float_as_uint(x), __float_as_uint(x), false, false);
  x = fmaxf(__uint_as_float(s[0]), __uint_as_float(s[1]));
  auto t = __builtin_amdgcn_permlane32_swap(__float_as_uint(x), __float_as_uint(x), false, false);
  return fmaxf(__uint_as_float(t[0]), __uint_as_float(t[1]));
}
__device__ __forceinline__ float wave_sum(float x) {
  x += dpp<XOR1>(x); x += dpp<XOR2>(x); x += dpp<XOR7>(x); x += dpp<ROR8>(x);
  return xrow_sum(x);
}
__device__ __forceinline__ float wave_max(float x) {
  x = fmaxf(x, dpp<XOR1>(x)); x = fmaxf(x, dpp<XOR2>(x)); x = fmaxf(x, dpp<XOR7>(x)); x = fmaxf(x, dpp<ROR8>(x));
  return xrow_max(x);
}
__device__ __forceinline__ float dot2(u32 a, u32 b, float c) {
  return __builtin_amdgcn_fdot2_f32_bf16(__builtin_bit_cast(bf16x2, a), __builtin_bit_cast(bf16x2, b), c, false);
}
typedef __amdgpu_buffer_rsrc_t rsrc_t;
__device__ __forceinline__ rsrc_t mk_rsrc(const void* p, u32 bytes) {
  return __builtin_amdgcn_make_buffer_rsrc((void*)p, 0, bytes, 0x00020000);
}
__device__ __forceinline__ u32x4 ld_nt(rsrc_t r, u32 voff, u32 soff) { return __builtin_amdgcn_raw_buffer_load_b128(r, voff, soff, AUX_NT); }
__device__ __forceinline__ u32x4 ld_pl(rsrc_t r, u32 voff, u32 soff) { return __builtin_amdgcn_raw_buffer_load_b128(r, voff, soff, 0); }
__device__ __forceinline__ u32x4 ld_sc(rsrc_t r, u32 voff, u32 soff) { return __builtin_amdgcn_raw_buffer_load_b128(r, voff, soff, AUX_SC1); }
__device__ __forceinline__ void st_unit(rsrc_t r, u32 voff, float a, float b, u32 tag) {
  u32x4 v = {__float_as_uint(a), tag, __float_as_uint(b), tag};
  __builtin_amdgcn_raw_buffer_store_b128(v, r, voff, 0, AUX_SC1);
}
__device__ __forceinline__ f32x4 mfma(u32x4 a, u32x4 b, f32x4 c) {
  return __builtin_amdgcn_mfma_f32_16x16x32_bf16(__builtin_bit_cast(bf16x8, a), __builtin_bit_cast(bf16x8, b), c, 0, 0, 0);
}
template <int N> __device__ __forceinline__ void waitv() { asm volatile("s_waitcnt vmcnt(%0)" :: "i"(N) : "memory"); }
#define CBAR() asm volatile("" ::: "memory")
#define SCHEDB() __builtin_amdgcn_sched_barrier(0)
__device__ __forceinline__ u32 lds_ld(volatile LAS u32* p) { return *p; }

__device__ __forceinline__ void tr8(const u32x4* W, u32x4* Bf) {
#pragma unroll
  for (int q = 0; q < 4; ++q) {
#pragma unroll
    for (int p = 0; p < 4; ++p) {
      Bf[2 * q][p] = __builtin_amdgcn_perm(W[2 * p + 1][q], W[2 * p][q], 0x05040100u);
      Bf[2 * q + 1][p] = __builtin_amdgcn_perm(W[2 * p + 1][q], W[2 * p][q], 0x07060302u);
    }
  }
}
__device__ __forceinline__ void epi(const f32x4* acc, int lane, float& e0, float& e1) {
  const bool lo = (lane & 8) == 0, g1 = (lane & 16) != 0, g2 = (lane & 32) != 0;
  float v[8];
#pragma unroll
  for (int j = 0; j < 8; ++j) {
    const float r0 = acc[j][0], r1 = acc[j][1], r2 = acc[j][2], r3 = acc[j][3];
    const float a = r0 + dpp<ROR8>(r1), b = r3 + dpp<ROR8>(r2);
    v[j] = lo ? a : b;
  }
  const float p0 = g1 ? v[2] : v[0], p1 = g1 ? v[6] : v[4], q0 = g1 ? v[3] : v[1], q1 = g1 ? v[7] : v[5];
  e0 = g2 ? p1 : p0;
  e1 = g2 ? q1 : q0;
}

__global__ void __launch_bounds__(NTHR, 3)
fused_decode(const u16* __restrict__ x, const u16* __restrict__ kc, const u16* __restrict__ vc,
             const u16* __restrict__ wq, const u16* __restrict__ wo, const u16* __restrict__ wg,
             const u16* __restrict__ wu, const u16* __restrict__ wd, u16* __restrict__ out, unsigned char* __restrict__ ws) {
  extern __shared__ __attribute__((aligned(16))) u32 lds_raw[];
  LAS u32* lds = (LAS u32*)lds_raw;
  const int tid = threadIdx.x, lane = tid & 63;
  const int wave = __builtin_amdgcn_readfirstlane(tid >> 6);
  {
    const u32 voff = ((blockIdx.x >> 3) * 4 + wave) * 1024 + lane * 16;
    asm volatile("s_cmp_eq_u32 %1, 0\n\ts_cbranch_scc0 .Lcpf_%=\n\t"
                 "s_getpc_b64 s[60:61]\n\ts_and_b32 s61, s61, 0xffff\n\ts_movk_i32 s62, %3\n\ts_mov_b32 s63, 0x20000\n\t"
                 "s_mov_b32 s59, m0\n\ts_mov_b32 m0, %2\n\ts_nop 0\n\tbuffer_load_dwordx4 %0, s[60:63], 0 offen lds\n\ts_mov_b32 m0, s59\n.Lcpf_%=:"
                 :: "v"(voff), "s"((blockIdx.x >> 5) | (wave >> 2)), "s"(16384 + wave * 1024), "i"(CODE_BYTES) : "s59", "s60", "s61", "s62", "s63", "scc", "memory");
  }
  const int g = blockIdx.x;
  const int h = (g & 7) * 4 + (g >> 6), j = (g >> 3) & 7;
  const int kq = j >> 1;
  const int nch = h < 12 ? 6 : 5, c0 = 5 * h + (h < 12 ? h : 12);
  const int sw = (wave - 2) & 3;
  const u32 kvoffb = ((u32)((j >> 2) * NH + h) * SK + (SK - WIN) + 256 * (j & 3) + 64 * sw) * (DH * 2);
  u32 o0 = (u32)kq * (128u * 8192u) + h * 256 + 128 * (j & 1), o1 = (u32)kq * (128u * 2752u) + 16 * c0;
  asm volatile("" : "+s"(o0), "+s"(o1));
  const u32 aoffb = wave >= 2 ? kvoffb : wave == 0 ? o0 : o1;
  const u32 strb = wave >= 2 ? 1024u : wave == 0 ? 8192u : 2752u;
  const u32 rstr = wave >= 2 ? 128u : 8u * strb;
  const u32 chmax = wave == 1 ? nch - 1 : 7;
  u32 avo = (lane >> 3) * rstr + min((u32)(lane & 7), chmax) * 16;
  asm volatile("" : "+v"(avo));
  SCHEDB();
  asm volatile("s_waitcnt lgkmcnt(0)" :: "s"(x), "s"(kc), "s"(vc), "s"(wq), "s"(wo), "s"(wg), "s"(wu), "s"(wd), "s"(out), "s"(ws) : "memory");
  const u16* abase = wave >= 2 ? kc : wave == 0 ? wq : (j & 1) ? wu : wg;
  const rsrc_t ra = mk_rsrc((const unsigned char*)abase + aoffb, 128u * strb);
  u32x4 R[16];
  if (wave < 6) {
#pragma unroll
    for (int t = 0; t < KWIN; ++t) { R[t] = ld_nt(ra, avo, t * strb); SCHEDB(); }
  }
  lds[tid & 31] = 0u;
  asm volatile("s_waitcnt lgkmcnt(0)" ::: "memory");
  SCHEDB();

  if (wave >= 2) {
    const int c = lane & 15;
    if (wave < 6) {
      asm volatile("s_barrier" ::: "memory");
      SCHEDB();
#pragma unroll
      for (int t = KWIN; t < 16; ++t) {
        waitv<KWIN - 1>();
        SCHEDB();
        R[t] = ld_nt(ra, avo, t * 1024);
        SCHEDB();
        if (t == KQT) { if (sw == 0 && lane == 0) *(volatile LAS u32*)(lds + L_KQ) = 1u; SCHEDB(); }
      }
      waitv<KSIG>();
      if (lane == 0) *(volatile LAS u32*)(lds + L_KI + sw) = 1u;
      for (u32 sp = 0; lds_ld(lds + L_QF) == 0u && sp < LSPIN_CAP; ++sp) __builtin_amdgcn_s_sleep(1);
      CBAR();
      const u32x4 q = *(LAS u32x4*)(lds + L_QPK + 4 * c);
      float xs[16];
#pragma unroll
      for (int t = 0; t < 16; ++t) {
        float d = dot2(R[t][0], q[0], 0.f);
        d = dot2(R[t][1], q[1], d); d = dot2(R[t][2], q[2], d); xs[t] = dot2(R[t][3], q[3], d);
      }
#define TR_STEP(HALF, CTRL, BIT) _Pragma("unroll") for (int u = 0; u < (HALF); ++u) { \
        const bool up_ = (c & (BIT)) != 0; const float keep_ = up_ ? xs[u + (HALF)] : xs[u], send_ = up_ ? xs[u] : xs[u + (HALF)]; \
        xs[u] = keep_ + dpp<CTRL>(send_); }
      TR_STEP(8, ROR8, 8) TR_STEP(4, XOR7, 4) TR_STEP(2, XOR2, 2) TR_STEP(1, XOR1, 1)
#undef TR_STEP
      const float sc = rbf(rbf(xs[0]) * 0.08838834764831845f);
      const float mw = wave_max(sc);
      if (lane == 0) { *(LAS u32x2*)(lds + L_MX + 2 * sw) = u32x2{__float_as_uint(mw), 1u}; }
      float M;
      for (u32 sp = 0;; ++sp) {
        CBAR();
        const u32x4 a = *(volatile LAS u32x4*)(lds + L_MX), bq = *(volatile LAS u32x4*)(lds + L_MX + 4);
        M = fmaxf(fmaxf(__uint_as_float(a[0]), __uint_as_float(a[2])), fmaxf(__uint_as_float(bq[0]), __uint_as_float(bq[2])));
        if ((a[1] & a[3] & bq[1] & bq[3]) != 0u || sp >= LSPIN_CAP) break;
      }
      const float p = __builtin_amdgcn_exp2f((sc - M) * 1.4426950408889634f);
      lds[L_P + 64 * sw + lane] = __float_as_uint(p);
      const float lw = wave_sum(p);
      if (lane == 0) lds[L_PL + sw] = __float_as_uint(lw);
      CBAR();
      if (lane == 0) *(volatile LAS u32*)(lds + L_PR + sw) = 1u;
      return;
    }
    const rsrc_t rv = mk_rsrc((const unsigned char*)vc + kvoffb, 16384);
    const u32 vo = lane * 16;
    u32x4 V[16];
    asm volatile("s_barrier" ::: "memory");
    for (u32 sp = 0; lds_ld(lds + L_KI + sw) == 0u && sp < LSPIN_CAP; ++sp) __builtin_amdgcn_s_sleep(2);
    SCHEDB();
#pragma unroll
    for (int t = 0; t < 16; ++t) {
      if (t >= VWIN) waitv<VWIN - 1>();
      SCHEDB();
      V[t] = ld_nt(rv, vo, t * 1024);
      SCHEDB();
    }
    if (lane == 0) *(volatile LAS u32*)(lds + L_VI + sw) = 1u;
    for (u32 sp = 0;; ++sp) {
      CBAR();
      const u32x4 a = *(volatile LAS u32x4*)(lds + L_PR);
      if ((a[0] & a[1] & a[2] & a[3]) != 0u || sp >= LSPIN_CAP) break;
      __builtin_amdgcn_s_sleep(1);
    }
    CBAR();
    const float p = __uint_as_float(lds[L_P + 64 * sw + lane]);
    u32x4 xst;
    {
      const u32x4 a = *(LAS u32x4*)(lds + L_MX), bq = *(LAS u32x4*)(lds + L_MX + 4), lq = *(LAS u32x4*)(lds + L_PL);
      const u32 tg = lds[L_TAG];
      const float Mcu = fmaxf(fmaxf(__uint_as_float(a[0]), __uint_as_float(a[2])), fmaxf(__uint_as_float(bq[0]), __uint_as_float(bq[2])));
      const float Lcu = (__uint_as_float(lq[0]) + __uint_as_float(lq[1])) + (__uint_as_float(lq[2]) + __uint_as_float(lq[3]));
      xst = u32x4{__float_as_uint(Mcu), tg, __float_as_uint(Lcu), tg};
      asm volatile("" : "+v"(xst));
    }
    const rsrc_t rws = mk_rsrc(ws, WS_END);
    const u32 xb = WS_X2 + (h * 8 + j) * 2048;
    float acc[8];
#pragma unroll
    for (int k = 0; k < 8; ++k) acc[k] = 0.f;
#define PV_STEP(T) { const float pb = dpp<0x150 + (T)>(p); \
      _Pragma("unroll") for (int k = 0; k < 4; ++k) { acc[2 * k] = fmaf(pb, bflo(V[T][k]), acc[2 * k]); acc[2 * k + 1] = fmaf(pb, bfhi(V[T][k]), acc[2 * k + 1]); } }
    PV_STEP(0) PV_STEP(1) PV_STEP(2) PV_STEP(3) PV_STEP(4) PV_STEP(5) PV_STEP(6) PV_STEP(7)
    PV_STEP(8) PV_STEP(9) PV_STEP(10) PV_STEP(11) PV_STEP(12) PV_STEP(13) PV_STEP(14) PV_STEP(15)
#undef PV_STEP
    float v1[4];
#pragma unroll
    for (int k = 0; k < 4; ++k) {
      auto sw2 = __builtin_amdgcn_permlane32_swap(__float_as_uint(acc[k]), __float_as_uint(acc[k + 4]), false, false);
      v1[k] = __uint_as_float(sw2[0]) + __uint_as_float(sw2[1]);
    }
    auto s0 = __builtin_amdgcn_permlane16_swap(__float_as_uint(v1[0]), __float_as_uint(v1[2]), false, false);
    auto s1 = __builtin_amdgcn_permlane16_swap(__float_as_uint(v1[1]), __float_as_uint(v1[3]), false, false);
    const float o0 = __uint_as_float(s0[0]) + __uint_as_float(s0[1]), o1 = __uint_as_float(s1[0]) + __uint_as_float(s1[1]);
    *(LAS f32x2*)(lds + L_PART + 128 * sw + 8 * c + 2 * (lane >> 4)) = f32x2{o0, o1};
    CBAR();
    u32 arr = 0;
    if (lane == 0) arr = __hip_atomic_fetch_add(lds + L_PC, 1u, __ATOMIC_RELAXED, __HIP_MEMORY_SCOPE_WORKGROUP);
    arr = __builtin_amdgcn_readfirstlane(arr);
    if (arr == 3u) {
      if (lane == 0) __builtin_amdgcn_raw_buffer_store_b128(xst, rws, xb + 1024, 0, AUX_SC1);
      CBAR();
      const f32x2 p0 = *(LAS f32x2*)(lds + L_PART + 2 * lane), p1 = *(LAS f32x2*)(lds + L_PART + 128 + 2 * lane),
                  p2 = *(LAS f32x2*)(lds + L_PART + 256 + 2 * lane), p3 = *(LAS f32x2*)(lds + L_PART + 384 + 2 * lane);
      st_unit(rws, xb + lane * 16, (p0[0] + p1[0]) + (p2[0] + p3[0]), (p0[1] + p1[1]) + (p2[1] + p3[1]), xst[1]);
      CBAR();
      if (lane == 0) *(volatile LAS u32*)(lds + L_X2) = 1u;
    }
    return;
  }

  __builtin_amdgcn_s_setprio(3);
  const int f = wave;
  const rsrc_t rws = mk_rsrc(ws, WS_END);
  const int m4 = lane & 3, gq = lane >> 4, rho = lane >> 3, chunk = lane & 7;
  const int ab = (m4 >> 1), ahi = m4 & 1;
  const rsrc_t rx = mk_rsrc(x, 2048);
  const u32x4 xs0 = ld_pl(rx, lane * 16, 0), xs1 = ld_pl(rx, lane * 16, 1024);
  const u32 xaoff = ab * 1024 + (128 * kq + 16 * gq + 8 * ahi) * 2;
  const u32x4 xa0 = ld_pl(rx, xaoff, 0), xa1 = ld_pl(rx, xaoff, 128);
  const u32 E = __builtin_amdgcn_raw_buffer_load_b32(rws, WS_E, 0, AUX_SC1);
  const u32 xfin = __builtin_amdgcn_raw_buffer_load_b32(rx, (lane & 1) * 1024 + (64 * j + 2 * h) * 2, 0, 0);
#pragma unroll
  for (int t = KWIN; t < 16; ++t) { R[t] = ld_nt(ra, avo, ((t >> 3) * 64 + (t & 7)) * strb); SCHEDB(); }
  asm volatile("s_barrier" ::: "memory");
  SCHEDB();
  float mb, rd;
  {
    float s0 = 0.f, s1 = 0.f;
#pragma unroll
    for (int k = 0; k < 4; ++k) { s0 += bflo(xs0[k]) + bfhi(xs0[k]); s1 += bflo(xs1[k]) + bfhi(xs1[k]); }
    const float mean0 = wave_sum(s0) * (1.f / 512.f), mean1 = wave_sum(s1) * (1.f / 512.f);
    float q0 = 0.f, q1 = 0.f;
#pragma unroll
    for (int k = 0; k < 4; ++k) {
      float a = bflo(xs0[k]) - mean0, bb = bfhi(xs0[k]) - mean0; q0 = fmaf(a, a, q0); q0 = fmaf(bb, bb, q0);
      a = bflo(xs1[k]) - mean1; bb = bfhi(xs1[k]) - mean1; q1 = fmaf(a, a, q1); q1 = fmaf(bb, bb, q1);
    }
    const float var0 = wave_sum(q0) * (1.f / 512.f), var1 = wave_sum(q1) * (1.f / 512.f);
    const float mean = ab ? mean1 : mean0, var = ab ? var1 : var0;
    mb = rbf(mean);
    rd = rbf(__builtin_amdgcn_sqrtf(rbf(rbf(var) + 1e-5f)));
  }
  const float rinv = __builtin_amdgcn_rcpf(rd);
  u32x4 A0, A1;
#pragma unroll
  for (int k = 0; k < 4; ++k) {
    float v[4] = {rbf(bflo(xa0[k]) - mb), rbf(bfhi(xa0[k]) - mb), rbf(bflo(xa1[k]) - mb), rbf(bfhi(xa1[k]) - mb)};
#pragma unroll
    for (int e = 0; e < 4; ++e) { const float q0 = v[e] * rinv; const float r = fmaf(-q0, rd, v[e]); v[e] = fmaf(r, rinv, q0); }
    A0[k] = pk2(v[0], v[1]); A1[k] = pk2(v[2], v[3]);
  }
  const u32 tag = E + 1u;
  float e0, e1;
  {
    f32x4 acc[8];
    u32x4 Bf[8];
    tr8(R, Bf);
#pragma unroll
    for (int t = 0; t < 8; ++t) acc[t] = mfma(A0, Bf[t], f32x4{0.f, 0.f, 0.f, 0.f});
    tr8(R + 8, Bf);
#pragma unroll
    for (int t = 0; t < 8; ++t) acc[t] = mfma(A1, Bf[t], acc[t]);
    epi(acc, lane, e0, e1);
  }
  const u32 uidx = ((lane >> 3) & 1) * 32 + 4 * (lane & 7) + (lane >> 4);
  st_unit(rws, WS_X1 + ((h * 8 + j) * 2 + f) * 1024 + uidx * 16, e0, e1, tag);
  if (f == 1) {
    float a0x, a0y, a1x, a1y;
    for (u32 sp = 0; lds_ld(lds + L_KI) == 0u && sp < LSPIN_CAP; ++sp) __builtin_amdgcn_s_sleep(2);
    for (u32 sp = 0;; ++sp) {
      CBAR();
      u32x4 u[8];
#pragma unroll
      for (int k = 0; k < 8; ++k) u[k] = ld_sc(rws, WS_X1 + (h * 16 + 1) * 1024 + lane * 16, k * 2048);
      bool ok = true;
#pragma unroll
      for (int k = 0; k < 8; ++k) ok = ok && u[k][1] == tag && u[k][3] == tag;
      a0x = (__uint_as_float(u[0][0]) + __uint_as_float(u[2][0])) + (__uint_as_float(u[4][0]) + __uint_as_float(u[6][0]));
      a0y = (__uint_as_float(u[0][2]) + __uint_as_float(u[2][2])) + (__uint_as_float(u[4][2]) + __uint_as_float(u[6][2]));
      a1x = (__uint_as_float(u[1][0]) + __uint_as_float(u[3][0])) + (__uint_as_float(u[5][0]) + __uint_as_float(u[7][0]));
      a1y = (__uint_as_float(u[1][2]) + __uint_as_float(u[3][2])) + (__uint_as_float(u[5][2]) + __uint_as_float(u[7][2]));
      if (__all(ok) || sp >= SPIN_CAP) break;
    }
    const float g0 = rbf(a0x), g1 = rbf(a0y), u0 = rbf(a1x), u1 = rbf(a1y);
    const float s0 = rbf(__builtin_amdgcn_rcpf(1.f + __builtin_amdgcn_exp2f(-g0 * 1.4426950408889634f)));
    const float s1 = rbf(__builtin_amdgcn_rcpf(1.f + __builtin_amdgcn_exp2f(-g1 * 1.4426950408889634f)));
    const u32 hp = (lane & 31) < 4 * nch ? pk2(s0 * u0, s1 * u1) : 0u;
    lds[L_HPK + lane] = hp;
    for (u32 sp = 0;; ++sp) {
      CBAR();
      const u32x4 a = *(volatile LAS u32x4*)(lds + L_VI);
      if ((a[0] & a[1] & a[2] & a[3]) != 0u || sp >= LSPIN_CAP) break;
      __builtin_amdgcn_s_sleep(2);
    }
    {
      const u16* pb = wd + (size_t)(8 * c0) * D + 64 * j;
      const int rr = rho < nch ? rho : nch - 1;
      const rsrc_t rb = mk_rsrc(pb, 64u * 1024u);
      const u32 vo = rr * 8192 + chunk * 16;
#pragma unroll
      for (int t = 0; t < 8; ++t) { R[t] = ld_nt(rb, vo, t * 1024); SCHEDB(); }
    }
    CBAR();
    const u32x4 Ah = *(LAS u32x4*)(lds + L_HPK + ab * 32 + 8 * gq + 4 * ahi);
    f32x4 acc[8];
    u32x4 Bf[8];
    tr8(R, Bf);
#pragma unroll
    for (int t = 0; t < 8; ++t) acc[t] = mfma(Ah, Bf[t], f32x4{0.f, 0.f, 0.f, 0.f});
    epi(acc, lane, e0, e1);
    *(LAS f32x2*)(lds + L_YF + 2 * lane) = f32x2{e0, e1};
    CBAR();
    if (lane == 0) *(volatile LAS u32*)(lds + L_FF) = 1u;
    return;
  }
  {
    float qa, qb;
    for (u32 sp = 0; lds_ld(lds + L_KQ) == 0u && sp < LSPIN_CAP; ++sp) __builtin_amdgcn_s_sleep(2);
    for (u32 sp = 0;; ++sp) {
      CBAR();
      u32x4 u[4];
#pragma unroll
      for (int k = 0; k < 4; ++k) u[k] = ld_sc(rws, WS_X1 + (h * 16 + 2 * (lane >> 5)) * 1024 + ((j >> 2) * 32 + (lane & 31)) * 16, k * 4096);
      bool ok = true;
#pragma unroll
      for (int k = 0; k < 4; ++k) ok = ok && u[k][1] == tag && u[k][3] == tag;
      qa = (__uint_as_float(u[0][0]) + __uint_as_float(u[1][0])) + (__uint_as_float(u[2][0]) + __uint_as_float(u[3][0]));
      qb = (__uint_as_float(u[0][2]) + __uint_as_float(u[1][2])) + (__uint_as_float(u[2][2]) + __uint_as_float(u[3][2]));
      if (__all(ok) || sp >= SPIN_CAP) break;
    }
    lds[L_QPK + lane] = pk2(qa, qb);
    if (lane == 0) lds[L_TAG] = tag;
    CBAR();
    if (lane == 0) *(volatile LAS u32*)(lds + L_QF) = 1u;
  }
  for (u32 sp = 0;; ++sp) {
    CBAR();
    const u32x4 a = *(volatile LAS u32x4*)(lds + L_VI);
    if ((a[0] & a[1] & a[2] & a[3]) != 0u || sp >= LSPIN_CAP) break;
    __builtin_amdgcn_s_sleep(2);
  }
  {
    const rsrc_t rb = mk_rsrc(wo + (size_t)(h * 128) * D + 64 * j, 128u * 1024u);
    const u32 vo = rho * 8192 + chunk * 16;
#pragma unroll
    for (int t = 0; t < 16; ++t) { R[t] = ld_nt(rb, vo, (t >> 3) * 65536 + (t & 7) * 1024); SCHEDB(); }
  }
  for (u32 sp = 0; lds_ld(lds + L_X2) == 0u && sp < LSPIN_CAP; ++sp) {}
  __builtin_amdgcn_s_sleep(X2D);
  u32x4 u[8], us;
#define X2_ISSUE() do { us = ld_sc(rws, WS_X2 + (h * 8 + (lane & 7)) * 2048 + 1024, 0); \
    _Pragma("unroll") for (int k = 0; k < 8; ++k) u[k] = ld_sc(rws, WS_X2 + h * 8 * 2048 + lane * 16, k * 2048); SCHEDB(); } while (0)
  X2_ISSUE();
  u32x4 B0[8], B1[8];
  tr8(R, B0); tr8(R + 8, B1);
#pragma unroll
  for (int t = 0; t < 8; ++t) asm volatile("" : "+v"(B0[t]), "+v"(B1[t]));
  for (u32 sp = 0; lds_ld(lds + L_FF) == 0u && sp < LSPIN_CAP; ++sp) {}
  CBAR();
  f32x2 yf = *(LAS f32x2*)(lds + L_YF + 2 * lane);
  u32 x3off = WS_X3 + ((j * 32 + 4 * (lane & 7) + (lane >> 4)) * 64 + 2 * h + ((lane >> 3) & 1)) * 16;
  asm volatile("" : "+v"(yf), "+v"(x3off));
  float coef;
  for (u32 sp = 0;; ++sp) {
    const float m = __uint_as_float(us[0]), l = __uint_as_float(us[2]);
    float Mb = fmaxf(m, dpp<XOR1>(m)); Mb = fmaxf(Mb, dpp<XOR2>(Mb));
    const float w = __builtin_amdgcn_exp2f((m - Mb) * 1.4426950408889634f);
    float L = w * l; L += dpp<XOR1>(L); L += dpp<XOR2>(L);
    coef = w * __builtin_amdgcn_rcpf(L);
    asm volatile("" : "+v"(coef));
    SCHEDB();
    bool ok = us[1] == tag && us[3] == tag;
#pragma unroll
    for (int k = 0; k < 8; ++k) ok = ok && u[k][1] == tag && u[k][3] == tag;
    if (__all(ok) || sp >= SPIN_CAP) break;
    CBAR();
    X2_ISSUE();
  }
#undef X2_ISSUE
  {
    float o00 = 0.f, o01 = 0.f, o10 = 0.f, o11 = 0.f;
#pragma unroll
    for (int k = 0; k < 4; ++k) {
      const float ca = __uint_as_float(__builtin_amdgcn_readlane(__float_as_uint(coef), k));
      const float cb = __uint_as_float(__builtin_amdgcn_readlane(__float_as_uint(coef), 4 + k));
      o00 = fmaf(ca, __uint_as_float(u[k][0]), o00); o01 = fmaf(ca, __uint_as_float(u[k][2]), o01);
      o10 = fmaf(cb, __uint_as_float(u[4 + k][0]), o10); o11 = fmaf(cb, __uint_as_float(u[4 + k][2]), o11);
    }
    lds[L_OPK + lane] = pk2(o00, o01);
    lds[L_OPK + 64 + lane] = pk2(o10, o11);
  }
  CBAR();
  {
    const u32x4 Ao0 = *(LAS u32x4*)(lds + L_OPK + ab * 64 + 8 * gq + 4 * ahi);
    const u32x4 Ao1 = *(LAS u32x4*)(lds + L_OPK + ab * 64 + 32 + 8 * gq + 4 * ahi);
    f32x4 acc[8];
#pragma unroll
    for (int t = 0; t < 8; ++t) acc[t] = mfma(Ao0, B0[t], f32x4{0.f, 0.f, 0.f, 0.f});
#pragma unroll
    for (int t = 0; t < 8; ++t) acc[t] = mfma(Ao1, B1[t], acc[t]);
    epi(acc, lane, e0, e1);
  }
  st_unit(rws, x3off, e0 + yf[0], e1 + yf[1], tag);
  __builtin_amdgcn_s_sleep(X3D);
  float y0, y1;
  for (u32 sp = 0;; ++sp) {
    CBAR();
    const u32x4 v = ld_sc(rws, WS_X3 + (j * 32 + h) * 1024 + lane * 16, 0);
    y0 = __uint_as_float(v[0]); y1 = __uint_as_float(v[2]);
    if (__all(v[1] == tag && v[3] == tag) || sp >= SPIN_CAP) break;
  }
  y0 += dpp<XOR2>(y0); y1 += dpp<XOR2>(y1);
  y0 += dpp<ROR4>(y0); y1 += dpp<ROR4>(y1);
  y0 += dpp<ROR8>(y0); y1 += dpp<ROR8>(y1);
  y0 = xrow_sum(y0); y1 = xrow_sum(y1);
  if (lane < 2) {
    const u32 o = pk2(bflo(xfin) + y0, bfhi(xfin) + y1);
    *(GAS u32*)(out + lane * D + 64 * j + 2 * h) = o;
  }
  if (g == 0 && lane == 0) __builtin_amdgcn_raw_buffer_store_b32(tag, rws, WS_E, 0, AUX_SC1);
}

extern "C" void kernel_launch(void* const* d_in, const int* in_sizes, int n_in,
                              void* d_out, int out_size, void* d_ws, size_t ws_size,
                              hipStream_t stream) {
  constexpr int LDS_BYTES = 84 * 1024;
  (void)hipFuncSetAttribute((const void*)fused_decode, hipFuncAttributeMaxDynamicSharedMemorySize, LDS_BYTES);
  fused_decode<<<256, NTHR, LDS_BYTES, stream>>>((const u16*)d_in[0], (const u16*)d_in[1], (const u16*)d_in[2], (const u16*)d_in[3],
      (const u16*)d_in[4], (const u16*)d_in[6], (const u16*)d_in[7], (const u16*)d_in[8], (u16*)d_out, (unsigned char*)d_ws);
}
```
